```python
import torch

def _softmax(x, axis=-1):
    return torch.softmax(x, dim=axis)
def _gelu(x):
    return torch.nn.functional.gelu(x, approximate="tanh")
def _silu(x):
    return torch.nn.functional.silu(x)
def _sigmoid(x):
    return torch.sigmoid(x)
def _layernorm(x, axis=-1, eps=1e-5):
    m = x.mean(dim=axis, keepdim=True)
    v = x.var(dim=axis, keepdim=True, unbiased=False)
    return (x - m) / torch.sqrt(v + eps)
def _scatter_add(tgt, idx, src, axis=0):
    return tgt.clone().index_add_(axis, idx, src)
def _segment_sum(src, seg, n):
    out = torch.zeros((n,) + src.shape[1:], dtype=src.dtype)
    return out.index_add_(0, seg, src)
def _pad(x, pw):
    flat = tuple(v for pair in reversed(pw) for v in pair)
    return torch.nn.functional.pad(x, flat)

kernel_name = 'interleave_layers__gqa_block_cached__gqa_block_cached__decode'

def setup_inputs():

    _w = (lambda a: (lambda t: t.to(torch.bfloat16) if t.is_floating_point() else t)(torch.as_tensor(a)))
    def _A_setup():
        B, Sq, D = 4, 1, 640
        Hq, Hkv, Sk, Dh = 32, 32, 4096, 128
        assert Hq % Hkv == 0, (Hq, Hkv)
        return {
            "x": _w((lambda *s: torch.randn(*s, dtype=torch.bfloat16) * 0.1)(B, Sq, D)),
            "k_cache": _w((lambda *s: torch.randn(*s, dtype=torch.bfloat16) * 0.8)(B, Hkv, Sk, Dh)),
            "v_cache": _w((lambda *s: torch.randn(*s, dtype=torch.bfloat16) * 3.2)(B, Hkv, Sk, Dh)),
            "wq": _w((lambda *s: torch.randn(*s, dtype=torch.bfloat16) * 0.8)(D, Hq * Dh) * (D ** -0.5)),
            "wo": _w((lambda *s: torch.randn(*s, dtype=torch.bfloat16) * 3.2)(Hq * Dh, D) * ((Hq * Dh) ** -0.5)),
        }
    _A_in = _A_setup()

    def _B_setup():
        B, Sq, D = 4, 1, 640
        Hq, Hkv, Sk, Dh = 32, 32, 4096, 128
        assert Hq % Hkv == 0, (Hq, Hkv)
        return {
            "x": _w((lambda *s: torch.randn(*s, dtype=torch.bfloat16) * 0.1)(B, Sq, D)),
            "k_cache": _w((lambda *s: torch.randn(*s, dtype=torch.bfloat16) * 1.6)(B, Hkv, Sk, Dh)),
            "v_cache": _w((lambda *s: torch.randn(*s, dtype=torch.bfloat16) * 1.6)(B, Hkv, Sk, Dh)),
            "wq": _w((lambda *s: torch.randn(*s, dtype=torch.bfloat16) * 1.6)(D, Hq * Dh) * (D ** -0.5)),
            "wo": _w((lambda *s: torch.randn(*s, dtype=torch.bfloat16) * 1.6)(Hq * Dh, D) * ((Hq * Dh) ** -0.5)),
        }
    _B_in = _B_setup()

    return {
        "A_x": _A_in["x"],
        "A_k_cache": _A_in["k_cache"],
        "A_v_cache": _A_in["v_cache"],
        "A_wq": _A_in["wq"],
        "A_wo": _A_in["wo"],
        "B_x": _B_in["x"],
        "B_k_cache": _B_in["k_cache"],
        "B_v_cache": _B_in["v_cache"],
        "B_wq": _B_in["wq"],
        "B_wo": _B_in["wo"],
    }

def reference(A_x, A_k_cache, A_v_cache, A_wq, A_wo, B_x, B_k_cache, B_v_cache, B_wq, B_wo):
    def _A_ref(x, k_cache, v_cache, wq, wo):
        B, Sq, D = x.shape
        Hq, Dh = 32, 128
        Hkv = k_cache.shape[1]
        rep = Hq // Hkv
        q = (lambda x, *d: x.permute(*d))((x @ wq).reshape(B, Sq, Hq, Dh), 0, 2, 1, 3)
        k = (lambda x, n, axis: torch.repeat_interleave(x, n, dim=axis))(k_cache, rep, axis=1)
        v = (lambda x, n, axis: torch.repeat_interleave(x, n, dim=axis))(v_cache, rep, axis=1)
        scores = torch.einsum("bhqd,bhkd->bhqk", q, k) * (Dh ** -0.5)
        p = _softmax(scores, axis=-1)
        o = torch.einsum("bhqk,bhkd->bhqd", p, v)
        return (lambda x, *d: x.permute(*d))(o, 0, 2, 1, 3).reshape(B, Sq, Hq * Dh) @ wo

    def _B_ref(x, k_cache, v_cache, wq, wo):
        B, Sq, D = x.shape
        Hq, Dh = 32, 128
        Hkv = k_cache.shape[1]
        rep = Hq // Hkv
        q = (lambda x, *d: x.permute(*d))((x @ wq).reshape(B, Sq, Hq, Dh), 0, 2, 1, 3)
        k = (lambda x, n, axis: torch.repeat_interleave(x, n, dim=axis))(k_cache, rep, axis=1)
        v = (lambda x, n, axis: torch.repeat_interleave(x, n, dim=axis))(v_cache, rep, axis=1)
        scores = torch.einsum("bhqd,bhkd->bhqk", q, k) * (Dh ** -0.5)
        p = _softmax(scores, axis=-1)
        o = torch.einsum("bhqk,bhkd->bhqd", p, v)
        return (lambda x, *d: x.permute(*d))(o, 0, 2, 1, 3).reshape(B, Sq, Hq * Dh) @ wo

    x = A_x
    x = x + _A_ref(_layernorm(x, axis=-1), A_k_cache, A_v_cache, A_wq, A_wo)
    x = x + _B_ref(_layernorm(x, axis=-1), B_k_cache, B_v_cache, B_wq, B_wo)
    x = x + _A_ref(_layernorm(x, axis=-1), A_k_cache, A_v_cache, A_wq, A_wo)
    x = x + _B_ref(_layernorm(x, axis=-1), B_k_cache, B_v_cache, B_wq, B_wo)
    return x
```

```cpp
#include <hip/hip_runtime.h>
#include <hip/hip_bf16.h>
#include <stdint.h>

#define ROUND_BF16 0

constexpr int NB = 4, DM = 640, NH = 32, SK = 4096, DH = 128, NQ = NH * DH;
constexpr int NSPLIT = 4;
using u32x4 = __attribute__((ext_vector_type(4))) unsigned;
using f32x4 = __attribute__((ext_vector_type(4))) float;
using bf16x2 = __attribute__((ext_vector_type(2))) __bf16;

__device__ __forceinline__ float bf_lo(unsigned u) { return __builtin_bit_cast(float, u << 16); }
__device__ __forceinline__ float bf_hi(unsigned u) { return __builtin_bit_cast(float, u & 0xffff0000u); }
__device__ __forceinline__ float rbf(float f) {
  return f;
}
__device__ __forceinline__ unsigned short f2bf(float f) {
  unsigned u = __builtin_bit_cast(unsigned, f);
  u += 0x7FFFu + ((u >> 16) & 1u);
  return (unsigned short)(u >> 16);
}
__device__ __forceinline__ float dot2(unsigned k, unsigned q, float acc) {
  return __builtin_amdgcn_fdot2_f32_bf16(__builtin_bit_cast(bf16x2, k), __builtin_bit_cast(bf16x2, q), acc, false);
}
template <int CTRL> __device__ __forceinline__ float dpp(float x) {
  return __builtin_bit_cast(float, __builtin_amdgcn_mov_dpp(__builtin_bit_cast(int, x), CTRL, 0xf, 0xf, true));
}
constexpr int XOR1 = 0xB1, XOR2 = 0x4E, XOR7 = 0x141, XOR8 = 0x128, ROR4 = 0x124;
__device__ __forceinline__ float xrow16_max(float x) {
  auto s = __builtin_amdgcn_permlane16_swap(__float_as_uint(x), __float_as_uint(x), false, false);
  x = fmaxf(__uint_as_float(s[0]), __uint_as_float(s[1]));
  auto t = __builtin_amdgcn_permlane32_swap(__float_as_uint(x), __float_as_uint(x), false, false);
  return fmaxf(__uint_as_float(t[0]), __uint_as_float(t[1]));
}
__device__ __forceinline__ float xrow16_sum(float x) {
  auto s = __builtin_amdgcn_permlane16_swap(__float_as_uint(x), __float_as_uint(x), false, false);
  x = __uint_as_float(s[0]) + __uint_as_float(s[1]);
  auto t = __builtin_amdgcn_permlane32_swap(__float_as_uint(x), __float_as_uint(x), false, false);
  return __uint_as_float(t[0]) + __uint_as_float(t[1]);
}

__global__ void __launch_bounds__(256) k_q(const unsigned short* x_in, const float* x_prev, const float* ypart, float* x_new,
                                           const unsigned short* wq, unsigned short* q_out, unsigned short* out_final, int mode) {
  __shared__ float xs[NB][DM];
  __shared__ float red[16][NB][128 + 4];
  __shared__ float st[NB][2];
  const int tid = threadIdx.x, h = blockIdx.x;
  for (int i = tid; i < NB * DM; i += 256) {
    float v;
    if (mode == 0) v = bf_lo(x_in[i]);
    else {
      float s = 0.f;
      for (int hh = 0; hh < NH; ++hh) s += ypart[hh * NB * DM + i];
      v = rbf(x_prev[i] + rbf(s));
    }
    xs[i / DM][i % DM] = v;
    if (h == 0) { x_new[i] = v; if (mode == 2) out_final[i] = f2bf(v); }
  }
  if (mode == 2) return;
  __syncthreads();
  const int w = tid >> 6, lane = tid & 63;
  {
    float s = 0.f;
    for (int i = lane; i < DM; i += 64) s += xs[w][i];
    for (int o = 32; o; o >>= 1) s += __shfl_xor(s, o);
    const float m = rbf(s / DM);
    const float mf = s / DM;
    float v = 0.f;
    for (int i = lane; i < DM; i += 64) { float d = xs[w][i] - mf; v += d * d; }
    for (int o = 32; o; o >>= 1) v += __shfl_xor(v, o);
    v = rbf(v / DM);
    const float sd = rbf(sqrtf(rbf(v + 1e-5f)));
    for (int i = lane; i < DM; i += 64) xs[w][i] = rbf(rbf(xs[w][i] - m) / sd);
  }
  __syncthreads();
  const int c = tid & 15, r = tid >> 4;
  float acc[NB][8];
  for (int b = 0; b < NB; ++b) for (int k = 0; k < 8; ++k) acc[b][k] = 0.f;
  for (int i = r; i < DM; i += 16) {
    u32x4 wv = *(const u32x4*)(wq + (size_t)i * NQ + h * DH + 8 * c);
#pragma unroll
    for (int b = 0; b < NB; ++b) {
      const float xv = xs[b][i];
#pragma unroll
      for (int k = 0; k < 4; ++k) { acc[b][2 * k] += xv * bf_lo(wv[k]); acc[b][2 * k + 1] += xv * bf_hi(wv[k]); }
    }
  }
  for (int b = 0; b < NB; ++b) for (int k = 0; k < 8; ++k) red[r][b][8 * c + k] = acc[b][k];
  __syncthreads();
  for (int i = tid; i < NB * DH; i += 256) {
    const int b = i >> 7, d = i & 127;
    float s = 0.f;
    for (int rr = 0; rr < 16; ++rr) s += red[rr][b][d];
    q_out[(size_t)b * NQ + h * DH + d] = f2bf(s);
  }
}

__global__ void __launch_bounds__(256) k_attn(const unsigned short* q, const unsigned short* kc, const unsigned short* vc, float* po, float* pml) {
  __shared__ __attribute__((aligned(16))) float lds[4 * 64 + 4 * 132 + 8];
  const int s = blockIdx.x, bh = blockIdx.y;
  const int tid = threadIdx.x, lane = tid & 63, wave = __builtin_amdgcn_readfirstlane(tid >> 6), r = lane >> 4, c = lane & 15;
  constexpr float C2 = 0.088388347648318440f * 1.4426950408889634f;
  constexpr int KPS = SK / NSPLIT;
  constexpr int KPW = KPS / 4;
  constexpr int NIT = KPW / 16;
  const u32x4 qv = *(const u32x4*)(q + (size_t)bh * DH + 8 * c);
  const size_t base = ((size_t)bh * SK + (size_t)s * KPS + (size_t)wave * KPW + r) * DH + 8 * c;
  const unsigned short* kp = kc + base;
  const unsigned short* vp = vc + base;
  u32x4 Kn[4], Vn[4];
#define ISSUE(i_) do { _Pragma("unroll") for (int t_ = 0; t_ < 4; ++t_) { Kn[t_] = __builtin_nontemporal_load((const u32x4*)(kp + (size_t)((i_) * 16 + t_ * 4) * DH)); Vn[t_] = __builtin_nontemporal_load((const u32x4*)(vp + (size_t)((i_) * 16 + t_ * 4) * DH)); } } while (0)
  float m = -INFINITY, l = 0.f, acc[8];
  for (int k = 0; k < 8; ++k) acc[k] = 0.f;
  ISSUE(0);
  for (int i = 0; i < NIT; ++i) {
    u32x4 Kc[4], Vc[4];
#pragma unroll
    for (int t = 0; t < 4; ++t) { Kc[t] = Kn[t]; Vc[t] = Vn[t]; }
    if (i + 1 < NIT) ISSUE(i + 1);
    float x[4];
#pragma unroll
    for (int t = 0; t < 4; ++t) { float d = 0.f;
#pragma unroll
      for (int k = 0; k < 4; ++k) d = dot2(Kc[t][k], qv[k], d);
      x[t] = d; }
#pragma unroll
    for (int t = 0; t < 4; ++t) { x[t] += dpp<XOR8>(x[t]); x[t] += dpp<XOR7>(x[t]); x[t] += dpp<XOR2>(x[t]); x[t] += dpp<XOR1>(x[t]); }
    float sc[4];
#pragma unroll
    for (int t = 0; t < 4; ++t) sc[t] = x[t] * C2;
    float pm = fmaxf(fmaxf(sc[0], sc[1]), fmaxf(sc[2], sc[3]));
    pm = xrow16_max(pm);
    const float mn = fmaxf(m, pm);
    const float alpha = __builtin_amdgcn_exp2f(m - mn);
    float p[4];
#pragma unroll
    for (int t = 0; t < 4; ++t) p[t] = __builtin_amdgcn_exp2f(sc[t] - mn);
    l = l * alpha + (p[0] + p[1] + p[2] + p[3]);
    m = mn;
#pragma unroll
    for (int k = 0; k < 8; ++k) acc[k] *= alpha;
#pragma unroll
    for (int t = 0; t < 4; ++t) {
#pragma unroll
      for (int k = 0; k < 4; ++k) { acc[2 * k] = fmaf(p[t], bf_lo(Vc[t][k]), acc[2 * k]); acc[2 * k + 1] = fmaf(p[t], bf_hi(Vc[t][k]), acc[2 * k + 1]); }
    }
  }
#pragma unroll
  for (int k = 0; k < 8; ++k) acc[k] = xrow16_sum(acc[k]);
  l = xrow16_sum(l);
  float* mrg = lds;
  if (r == 0) {
    for (int k = 0; k < 8; ++k) mrg[wave * 132 + 4 + 8 * c + k] = acc[k];
    if (c == 0) { mrg[wave * 132] = m; mrg[wave * 132 + 1] = l; }
  }
  __syncthreads();
  if (tid < 128) {
    float M = -INFINITY;
    for (int w = 0; w < 4; ++w) M = fmaxf(M, mrg[w * 132]);
    float L = 0.f, o = 0.f;
    for (int w = 0; w < 4; ++w) { const float wt = __builtin_amdgcn_exp2f(mrg[w * 132] - M); L += wt * mrg[w * 132 + 1]; o += wt * mrg[w * 132 + 4 + tid]; }
    po[((size_t)bh * NSPLIT + s) * DH + tid] = o;
    if (tid == 0) { pml[((size_t)bh * NSPLIT + s) * 2] = M; pml[((size_t)bh * NSPLIT + s) * 2 + 1] = L; }
  }
}

__global__ void __launch_bounds__(256) k_oproj(const float* po, const float* pml, const unsigned short* wo, float* ypart) {
  __shared__ float os[NB][DH];
  __shared__ float red[16][NB][128 + 4];
  const int tid = threadIdx.x, h = blockIdx.x, cc = blockIdx.y;
  for (int i = tid; i < NB * DH; i += 256) {
    const int b = i >> 7, d = i & 127, bh = b * NH + h;
    float M = -INFINITY;
    for (int s = 0; s < NSPLIT; ++s) M = fmaxf(M, pml[(bh * NSPLIT + s) * 2]);
    float L = 0.f, o = 0.f;
    for (int s = 0; s < NSPLIT; ++s) { const float wt = __builtin_amdgcn_exp2f(pml[(bh * NSPLIT + s) * 2] - M); L += wt * pml[(bh * NSPLIT + s) * 2 + 1]; o += wt * po[((size_t)bh * NSPLIT + s) * DH + d]; }
    os[b][d] = rbf(o / L);
  }
  __syncthreads();
  const int c = tid & 15, r = tid >> 4;
  float acc[NB][8];
  for (int b = 0; b < NB; ++b) for (int k = 0; k < 8; ++k) acc[b][k] = 0.f;
  for (int i = r; i < DH; i += 16) {
    u32x4 wv = *(const u32x4*)(wo + (size_t)(h * DH + i) * DM + cc * 128 + 8 * c);
#pragma unroll
    for (int b = 0; b < NB; ++b) {
      const float xv = os[b][i];
#pragma unroll
      for (int k = 0; k < 4; ++k) { acc[b][2 * k] += xv * bf_lo(wv[k]); acc[b][2 * k + 1] += xv * bf_hi(wv[k]); }
    }
  }
  for (int b = 0; b < NB; ++b) for (int k = 0; k < 8; ++k) red[r][b][8 * c + k] = acc[b][k];
  __syncthreads();
  for (int i = tid; i < NB * 128; i += 256) {
    const int b = i >> 7, d = i & 127;
    float s = 0.f;
    for (int rr = 0; rr < 16; ++rr) s += red[rr][b][d];
    ypart[(size_t)h * NB * DM + b * DM + cc * 128 + d] = s;
  }
}

extern "C" void kernel_launch(void* const* d_in, const int* in_sizes, int n_in, void* d_out, int out_size, void* d_ws, size_t ws_size, hipStream_t stream) {
  const unsigned short* x = (const unsigned short*)d_in[0];
  const unsigned short* kc[2] = {(const unsigned short*)d_in[1], (const unsigned short*)d_in[6]};
  const unsigned short* vc[2] = {(const unsigned short*)d_in[2], (const unsigned short*)d_in[7]};
  const unsigned short* wq[2] = {(const unsigned short*)d_in[3], (const unsigned short*)d_in[8]};
  const unsigned short* wo[2] = {(const unsigned short*)d_in[4], (const unsigned short*)d_in[9]};
  char* ws = (char*)d_ws;
  float* xa = (float*)(ws);
  float* xb = (float*)(ws + 16384);
  unsigned short* q = (unsigned short*)(ws + 32768);
  float* pml = (float*)(ws + 65536);
  float* po = (float*)(ws + 131072);
  float* ypart = (float*)(ws + (1 << 20));
  float* xs[2] = {xa, xb};
  for (int l = 0; l < 4; ++l) {
    const int s = l & 1;
    k_q<<<NH, 256, 0, stream>>>(x, xs[(l + 1) & 1], ypart, xs[l & 1], wq[s], q, (unsigned short*)d_out, l == 0 ? 0 : 1);
    k_attn<<<dim3(NSPLIT, NB * NH), 256, 0, stream>>>(q, kc[s], vc[s], po, pml);
    k_oproj<<<dim3(NH, 5), 256, 0, stream>>>(po, pml, wo[s], ypart);
  }
  k_q<<<1, 256, 0, stream>>>(x, xs[1], ypart, xs[0], wq[0], q, (unsigned short*)d_out, 2);
}
```

```cpp
#include <hip/hip_runtime.h>
#include <stdint.h>

struct KArgs { const void* p[11]; };

__global__ void __launch_bounds__(512, 2) decode4_kernel(KArgs args) {
  __shared__ __attribute__((aligned(16))) char lds[163840];
  auto ka = __builtin_amdgcn_kernarg_segment_ptr();
  const int wid = __builtin_amdgcn_readfirstlane(threadIdx.x >> 6);
  asm volatile("; lds base %0" :: "s"((unsigned)(uintptr_t)(__attribute__((address_space(3))) char*)lds));
  asm volatile(
R"ASM(
  s_load_dwordx16 s[16:31], %0, 0x0
  s_load_dwordx4 s[32:35], %0, 0x40
  s_load_dwordx2 s[36:37], %0, 0x50
  s_mov_b32 s65, %2
  s_and_b32 s72, %1, 7
  s_lshr_b32 s73, %1, 3
  s_lshl_b32 s99, %1, 10
  s_and_b32 s74, s73, 3
  s_lshl_b32 s66, s72, 2
  s_add_u32 s66, s66, s74
  s_lshr_b32 s67, s73, 2
  s_waitcnt lgkmcnt(0)
  s_mov_b32 s56, s16
  s_and_b32 s57, s17, 0xffff
  s_mov_b32 s58, 5120
  s_mov_b32 s59, 0x20000
  s_lshl_b32 s72, s66, 20
  s_lshl_b32 s73, s67, 17
  s_add_u32 s8, s72, s73
  s_sub_u32 s9, 134217728, s8
  s_add_u32 s16, s18, s8
  s_addc_u32 s17, s19, 0
  s_and_b32 s17, s17, 0xffff
  s_mov_b32 s18, s9
  s_mov_b32 s19, 0x20000
  s_mov_b32 s64, 0
  s_mov_b32 s68, 0
  s_mov_b32 s69, 0
  v_mbcnt_lo_u32_b32 v9, -1, 0
  v_mbcnt_hi_u32_b32 v9, -1, v9
  s_lshl_b32 s72, s65, 10
  v_lshlrev_b32 v0, 4, v9
  v_add_u32 v0, s72, v0
  v_bfe_u32 v10, v9, 2, 2
  v_lshlrev_b32 v10, 8, v10
  v_lshrrev_b32 v11, 4, v9
  v_lshl_add_u32 v10, v11, 6, v10
  v_and_b32 v11, 3, v9
  v_lshl_add_u32 v10, v11, 4, v10
  v_add_u32 v1, s72, v10
  s_mov_b32 s48, s36
  s_and_b32 s49, s37, 0xffff
  s_mov_b32 s50, 6950912
  s_mov_b32 s51, 0x20000
  s_mov_b32 s52, s34
  s_and_b32 s53, s35, 0xffff
  s_mov_b32 s54, 5120
  s_mov_b32 s55, 0x20000
  s_mul_i32 s72, s67, 655360
  s_lshl_b32 s73, s66, 8
  s_add_u32 s72, s72, s73
  s_add_u32 s40, s22, s72
  s_addc_u32 s41, s23, 0
  s_and_b32 s41, s41, 0xffff
  s_mov_b32 s42, 647424
  s_mov_b32 s43, 0x20000
  s_add_u32 s44, s30, s72
  s_addc_u32 s45, s31, 0
  s_and_b32 s45, s45, 0xffff
  s_mov_b32 s46, 647424
  s_mov_b32 s47, 0x20000
  s_mul_i32 s72, s66, 163840
  s_mul_i32 s73, s67, 160
  s_add_u32 s72, s72, s73
  s_sub_u32 s73, 5242880, s72
  s_add_u32 s36, s32, s72
  s_addc_u32 s37, s33, 0
  s_and_b32 s37, s37, 0xffff
  s_mov_b32 s38, s73
  s_mov_b32 s39, 0x20000
  s_add_u32 s32, s24, s72
  s_addc_u32 s33, s25, 0
  s_and_b32 s33, s33, 0xffff
  s_mov_b32 s34, s73
  s_mov_b32 s35, 0x20000
  s_mov_b64 s[74:75], s[26:27]
  s_mov_b64 s[76:77], s[20:21]
  s_add_u32 s28, s28, s8
  s_addc_u32 s29, s29, 0
  s_and_b32 s29, s29, 0xffff
  s_mov_b32 s30, s9
  s_mov_b32 s31, 0x20000
  s_add_u32 s20, s76, s8
  s_addc_u32 s21, s77, 0
  s_and_b32 s21, s21, 0xffff
  s_mov_b32 s22, s9
  s_mov_b32 s23, 0x20000
  s_add_u32 s24, s74, s8
  s_addc_u32 s25, s75, 0
  s_and_b32 s25, s25, 0xffff
  s_mov_b32 s26, s9
  s_mov_b32 s27, 0x20000
  s_mov_b32 s80, 0xaaaaaaaa
  s_mov_b32 s81, 0xaaaaaaaa
  s_mov_b32 s82, 0xcccccccc
  s_mov_b32 s83, 0xcccccccc
  s_mov_b32 s84, 0xf000f
  s_mov_b32 s85, 0xf000f
  s_mov_b32 s86, 0xf000f0
  s_mov_b32 s87, 0xf000f0
  s_mov_b32 s88, 0xf000f00
  s_mov_b32 s89, 0xf000f00
  s_mov_b32 s90, 0xf000f000
  s_mov_b32 s91, 0xf000f000
  s_mov_b32 s92, 0x3e0293ee
  s_mov_b32 s70, 147456
  s_mov_b32 s71, 0
  s_lshl_b32 s72, s66, 3
  s_add_u32 s72, s72, s67
  s_mul_i32 s96, s72, 1024
  s_add_u32 s98, s96, 327680
  s_lshl_b32 s72, s67, 5
  s_add_u32 s72, s72, s66
  s_mul_i32 s97, s72, 256
  s_add_u32 s97, s97, 262144
  v_mbcnt_lo_u32_b32 v9, -1, 0
  v_mbcnt_hi_u32_b32 v9, -1, v9
  v_and_b32 v10, 15, v9
  v_and_b32 v11, 1, v10
  v_cmp_eq_u32 vcc, 1, v11
  v_mov_b32 v12, 0xffff
  v_mov_b32 v13, 0xffff0000
  v_cndmask_b32 v11, v12, v13, vcc
  v_lshrrev_b32 v10, 1, v10
  v_cmp_eq_u32 vcc, 0, v10
  s_nop 1
  v_cndmask_b32 v2, 0, v11, vcc
  v_cmp_eq_u32 vcc, 1, v10
  s_nop 1
  v_cndmask_b32 v3, 0, v11, vcc
  v_cmp_eq_u32 vcc, 2, v10
  s_nop 1
  v_cndmask_b32 v4, 0, v11, vcc
  v_cmp_eq_u32 vcc, 3, v10
  s_nop 1
  v_cndmask_b32 v5, 0, v11, vcc
  v_mov_b32 v10, 0
  v_mov_b32 v11, 0
  v_mov_b32 v12, 0
  v_mov_b32 v13, 0
  v_mov_b32 v9, 155776
  ds_write_b128 v9, v[10:13]
  v_mov_b32 v136, 0
  v_mov_b32 v137, 0
  v_mov_b32 v138, 0
  v_mov_b32 v139, 0
  v_mov_b32 v12, 0
  s_mov_b32 s72, 5898240
  buffer_load_dwordx2 v[160:161], v12, s[48:51], s72 offen sc1
  v_bfe_u32 v22, v0, 9, 1
  v_mul_u32_u24 v22, 393216, v22
  v_bfe_u32 v23, v0, 8, 1
  v_lshl_add_u32 v22, v23, 16, v22
  v_and_b32 v23, 0xf0, v0
  v_add_u32 v22, v22, v23
  s_lshl_b32 s73, s65, 13
  s_add_u32 s74, s73, 0
  buffer_load_dwordx4 v[28:31], v22, s[40:43], s74 offen
  s_add_u32 s74, s73, 131072
  buffer_load_dwordx4 v[32:35], v22, s[40:43], s74 offen
  s_add_u32 s74, s73, 262144
  buffer_load_dwordx4 v[36:39], v22, s[40:43], s74 offen
  v_bfe_u32 v158, v0, 4, 4
  v_bfe_u32 v12, v0, 9, 1
  v_mul_u32_u24 v12, 3, v12
  v_add_u32 v158, v158, v12
  v_bfe_u32 v12, v0, 8, 1
  v_lshl_or_b32 v158, v158, 1, v12
  v_lshlrev_b32 v12, 4, v158
  s_mul_i32 s76, s67, 160
  s_lshl_b32 s73, s65, 1
  s_add_u32 s76, s76, s73
  s_add_u32 s72, s76, 0
  buffer_load_ushort v152, v12, s[56:59], s72 offen
  s_add_u32 s72, s76, 1280
  buffer_load_ushort v153, v12, s[56:59], s72 offen
  s_add_u32 s72, s76, 2560
  buffer_load_ushort v154, v12, s[56:59], s72 offen
  s_add_u32 s72, s76, 3840
  buffer_load_ushort v155, v12, s[56:59], s72 offen
  v_bfe_u32 v22, v0, 9, 1
  v_mul_u32_u24 v22, 393216, v22
  v_bfe_u32 v23, v0, 8, 1
  v_lshl_add_u32 v22, v23, 16, v22
  v_and_b32 v23, 0xf0, v0
  v_add_u32 v22, v22, v23
  s_lshl_b32 s73, s65, 13
  s_add_u32 s74, s73, 0
  buffer_load_dwordx4 v[140:143], v22, s[44:47], s74 offen
  s_add_u32 s74, s73, 131072
  buffer_load_dwordx4 v[144:147], v22, s[44:47], s74 offen
  s_add_u32 s74, s73, 262144
  buffer_load_dwordx4 v[148:151], v22, s[44:47], s74 offen
  s_lshl_b32 s74, s65, 10
  s_add_u32 s74, s74, 0
  s_add_u32 s74, s74, s69
  s_mov_b32 m0, s74
  s_add_u32 s75, s68, 0
  buffer_load_dwordx4 v1, s[16:19], s75 offen nt lds
  s_lshl_b32 s74, s65, 10
)ASM"
R"ASM(
  s_add_u32 s74, s74, 8192
  s_add_u32 s74, s74, s69
  s_mov_b32 m0, s74
  s_add_u32 s75, s68, 8192
  buffer_load_dwordx4 v1, s[16:19], s75 offen nt lds
  s_lshl_b32 s74, s65, 10
  s_add_u32 s74, s74, 16384
  s_add_u32 s74, s74, s69
  s_mov_b32 m0, s74
  s_add_u32 s75, s68, 16384
  buffer_load_dwordx4 v1, s[16:19], s75 offen nt lds
  s_lshl_b32 s74, s65, 10
  s_add_u32 s74, s74, 24576
  s_add_u32 s74, s74, s69
  s_mov_b32 m0, s74
  s_add_u32 s75, s68, 24576
  buffer_load_dwordx4 v1, s[16:19], s75 offen nt lds
  s_lshl_b32 s74, s65, 10
  s_add_u32 s74, s74, 32768
  s_add_u32 s74, s74, s69
  s_mov_b32 m0, s74
  s_add_u32 s75, s68, 32768
  buffer_load_dwordx4 v1, s[16:19], s75 offen nt lds
  s_lshl_b32 s74, s65, 10
  s_add_u32 s74, s74, 40960
  s_add_u32 s74, s74, s69
  s_mov_b32 m0, s74
  s_add_u32 s75, s68, 40960
  buffer_load_dwordx4 v1, s[16:19], s75 offen nt lds
  s_lshl_b32 s74, s65, 10
  s_add_u32 s74, s74, 49152
  s_add_u32 s74, s74, s69
  s_mov_b32 m0, s74
  s_add_u32 s75, s68, 49152
  buffer_load_dwordx4 v1, s[16:19], s75 offen nt lds
  s_lshl_b32 s74, s65, 10
  s_add_u32 s74, s74, 57344
  s_add_u32 s74, s74, s69
  s_mov_b32 m0, s74
  s_add_u32 s75, s68, 57344
  buffer_load_dwordx4 v1, s[16:19], s75 offen nt lds
  v_bfe_u32 v9, v0, 4, 6
  v_lshlrev_b32 v10, 4, v9
  s_cmp_lg_u32 s65, 0
  s_nop 0
  s_cbranch_scc1 .pw1
  s_add_u32 s72, s96, 0
  buffer_store_dwordx4 v[136:139], v10, s[48:51], s72 offen sc1
  s_add_u32 s72, s96, 589824
  buffer_store_dwordx4 v[136:139], v10, s[48:51], s72 offen sc1
  s_add_u32 s72, s96, 1179648
  buffer_store_dwordx4 v[136:139], v10, s[48:51], s72 offen sc1
  s_add_u32 s72, s96, 1769472
  buffer_store_dwordx4 v[136:139], v10, s[48:51], s72 offen sc1
  s_nop 1
.pw1:
  s_cmp_lg_u32 s65, 1
  s_cbranch_scc1 .pw2
  s_add_u32 s72, s97, 0
  s_mov_b32 exec_lo, 0xffff
  s_mov_b32 exec_hi, 0x0
  buffer_store_dwordx4 v[136:139], v10, s[48:51], s72 offen sc1
  s_mov_b64 exec, -1
  s_add_u32 s72, s97, 589824
  s_mov_b32 exec_lo, 0xffff
  s_mov_b32 exec_hi, 0x0
  buffer_store_dwordx4 v[136:139], v10, s[48:51], s72 offen sc1
  s_mov_b64 exec, -1
  s_add_u32 s72, s97, 1179648
  s_mov_b32 exec_lo, 0xffff
  s_mov_b32 exec_hi, 0x0
  buffer_store_dwordx4 v[136:139], v10, s[48:51], s72 offen sc1
  s_mov_b64 exec, -1
  s_add_u32 s72, s97, 1769472
  s_mov_b32 exec_lo, 0xffff
  s_mov_b32 exec_hi, 0x0
  buffer_store_dwordx4 v[136:139], v10, s[48:51], s72 offen sc1
  s_mov_b64 exec, -1
  s_nop 0
.pw2:
  s_cmp_lg_u32 s65, 2
  s_cbranch_scc1 .pw3
  s_add_u32 s72, s98, 0
  buffer_store_dwordx4 v[136:139], v10, s[48:51], s72 offen sc1
  s_add_u32 s72, s98, 589824
  buffer_store_dwordx4 v[136:139], v10, s[48:51], s72 offen sc1
  s_add_u32 s72, s98, 1179648
  buffer_store_dwordx4 v[136:139], v10, s[48:51], s72 offen sc1
  s_add_u32 s72, s98, 1769472
  buffer_store_dwordx4 v[136:139], v10, s[48:51], s72 offen sc1
  s_nop 1
.pw3:
  s_cmp_gt_u32 s65, 5
  s_cbranch_scc1 .pw4
  s_add_u32 s72, s65, 15
  s_cmp_eq_u32 s65, 0
  s_cselect_b32 s72, 20, s72
  s_cmp_eq_u32 s65, 5
  s_cselect_b32 s72, 21, s72
  s_sub_u32 s72, s72, 12
  s_mul_i32 s72, s72, 589824
  s_add_u32 s72, s72, s98
  buffer_store_dwordx4 v[136:139], v10, s[48:51], s72 offen sc1
  s_nop 1
.pw4:
  s_add_u32 s75, s68, 65536
  buffer_load_dwordx4 v[40:43], v1, s[16:19], s75 offen nt
  s_add_u32 s75, s68, 73728
  buffer_load_dwordx4 v[44:47], v1, s[16:19], s75 offen nt
  s_add_u32 s75, s68, 81920
  buffer_load_dwordx4 v[48:51], v1, s[16:19], s75 offen nt
  s_add_u32 s75, s68, 90112
  buffer_load_dwordx4 v[52:55], v1, s[16:19], s75 offen nt
.Lbatch:
  s_cmp_eq_u32 s64, 0
  s_cbranch_scc0 .c18
  s_waitcnt vmcnt(12)
  v_readfirstlane_b32 s60, v160
  v_readfirstlane_b32 s61, v161
  v_cmp_gt_u32 vcc, 10, v158
  v_mov_b32 v159, 0x3f803f80
  s_nop 0
  v_cndmask_b32 v159, 0, v159, vcc
  v_lshl_or_b32 v152, v152, 16, v152
  v_cndmask_b32 v152, 0, v152, vcc
  v_lshl_or_b32 v153, v153, 16, v153
  v_cndmask_b32 v153, 0, v153, vcc
  v_lshl_or_b32 v154, v154, 16, v154
  v_cndmask_b32 v154, 0, v154, vcc
  v_lshl_or_b32 v155, v155, 16, v155
  v_cndmask_b32 v155, 0, v155, vcc
  v_and_b32_dpp v10, v159, v2 row_newbcast:0 row_mask:0xf bank_mask:0xf
  v_and_b32_dpp v11, v159, v3 row_newbcast:0 row_mask:0xf bank_mask:0xf
  v_and_b32_dpp v12, v159, v4 row_newbcast:0 row_mask:0xf bank_mask:0xf
  v_and_b32_dpp v13, v159, v5 row_newbcast:0 row_mask:0xf bank_mask:0xf
  v_and_b32_dpp v14, v159, v2 row_newbcast:1 row_mask:0xf bank_mask:0xf
  v_and_b32_dpp v15, v159, v3 row_newbcast:1 row_mask:0xf bank_mask:0xf
  v_and_b32_dpp v16, v159, v4 row_newbcast:1 row_mask:0xf bank_mask:0xf
  v_and_b32_dpp v17, v159, v5 row_newbcast:1 row_mask:0xf bank_mask:0xf
  v_mfma_f32_16x16x32_bf16 v[20:23], v[28:31], v[10:13], 0
  v_and_b32_dpp v10, v159, v2 row_newbcast:2 row_mask:0xf bank_mask:0xf
  v_and_b32_dpp v11, v159, v3 row_newbcast:2 row_mask:0xf bank_mask:0xf
  v_and_b32_dpp v12, v159, v4 row_newbcast:2 row_mask:0xf bank_mask:0xf
  v_and_b32_dpp v13, v159, v5 row_newbcast:2 row_mask:0xf bank_mask:0xf
  v_mfma_f32_16x16x32_bf16 v[20:23], v[32:35], v[14:17], v[20:23]
  v_and_b32 v9, 0xf0, v0
  v_lshlrev_b32 v9, 2, v9
  v_bfe_u32 v19, v0, 8, 2
  v_lshl_or_b32 v9, v19, 4, v9
  s_lshl_b32 s72, s65, 9
  s_add_u32 s72, s72, 65536
  v_add_u32 v9, s72, v9
  v_mfma_f32_16x16x32_bf16 v[20:23], v[36:39], v[10:13], v[20:23]
  s_mov_b32 exec_lo, 0xff00ff
  s_mov_b32 exec_hi, 0xff00ff
  s_nop 5
  ds_write_b128 v9, v[20:23]
  s_mov_b64 exec, -1
  v_and_b32_dpp v10, v152, v2 row_newbcast:0 row_mask:0xf bank_mask:0xf
  v_and_b32_dpp v11, v152, v3 row_newbcast:0 row_mask:0xf bank_mask:0xf
  v_and_b32_dpp v12, v152, v4 row_newbcast:0 row_mask:0xf bank_mask:0xf
  v_and_b32_dpp v13, v152, v5 row_newbcast:0 row_mask:0xf bank_mask:0xf
  v_and_b32_dpp v14, v152, v2 row_newbcast:1 row_mask:0xf bank_mask:0xf
  v_and_b32_dpp v15, v152, v3 row_newbcast:1 row_mask:0xf bank_mask:0xf
  v_and_b32_dpp v16, v152, v4 row_newbcast:1 row_mask:0xf bank_mask:0xf
  v_and_b32_dpp v17, v152, v5 row_newbcast:1 row_mask:0xf bank_mask:0xf
  v_mfma_f32_16x16x32_bf16 v[20:23], v[28:31], v[10:13], 0
  v_and_b32_dpp v10, v152, v2 row_newbcast:2 row_mask:0xf bank_mask:0xf
  v_and_b32_dpp v11, v152, v3 row_newbcast:2 row_mask:0xf bank_mask:0xf
  v_and_b32_dpp v12, v152, v4 row_newbcast:2 row_mask:0xf bank_mask:0xf
  v_and_b32_dpp v13, v152, v5 row_newbcast:2 row_mask:0xf bank_mask:0xf
  v_mfma_f32_16x16x32_bf16 v[20:23], v[32:35], v[14:17], v[20:23]
  v_and_b32 v9, 0xf0, v0
  v_lshlrev_b32 v9, 2, v9
  v_bfe_u32 v19, v0, 8, 2
  v_lshl_or_b32 v9, v19, 4, v9
  s_lshl_b32 s72, s65, 9
  s_add_u32 s72, s72, 69760
  v_add_u32 v9, s72, v9
  v_mfma_f32_16x16x32_bf16 v[20:23], v[36:39], v[10:13], v[20:23]
  s_mov_b32 exec_lo, 0xff00ff
  s_mov_b32 exec_hi, 0xff00ff
  s_nop 5
  ds_write_b128 v9, v[20:23]
  s_mov_b64 exec, -1
  v_and_b32 v12, 0xffff0000, v152
  s_mov_b32 s78, 0x70007
  s_mov_b32 s79, 0x30003
  v_cndmask_b32 v16, 0, v12, s[78:79]
  v_mul_f32 v17, v16, v16
  s_nop 0
  v_add_f32_dpp v16, v16, v16 quad_perm:[1,0,3,2] row_mask:0xf bank_mask:0xf
  v_add_f32_dpp v17, v17, v17 quad_perm:[1,0,3,2] row_mask:0xf bank_mask:0xf
  s_nop 0
  v_add_f32_dpp v16, v16, v16 quad_perm:[2,3,0,1] row_mask:0xf bank_mask:0xf
  v_add_f32_dpp v17, v17, v17 quad_perm:[2,3,0,1] row_mask:0xf bank_mask:0xf
  s_nop 1
  v_permlane16_swap_b32 v16, v17
  v_add_f32 v16, v16, v17
  v_mov_b32 v18, v16
)ASM"
R"ASM(
  s_nop 1
  v_permlane32_swap_b32 v16, v18
  v_add_f32 v16, v16, v18
  s_lshl_b32 s72, s65, 3
  s_add_u32 s72, s72, 69760
  v_bfe_u32 v19, v0, 8, 1
  v_lshlrev_b32 v19, 2, v19
  v_add_u32 v19, s72, v19
  s_mov_b32 exec_lo, 0x10001
  s_mov_b32 exec_hi, 0x0
  ds_write_b32 v19, v16 offset:4096
  s_mov_b64 exec, -1
  v_and_b32_dpp v10, v153, v2 row_newbcast:0 row_mask:0xf bank_mask:0xf
  v_and_b32_dpp v11, v153, v3 row_newbcast:0 row_mask:0xf bank_mask:0xf
  v_and_b32_dpp v12, v153, v4 row_newbcast:0 row_mask:0xf bank_mask:0xf
  v_and_b32_dpp v13, v153, v5 row_newbcast:0 row_mask:0xf bank_mask:0xf
  v_and_b32_dpp v14, v153, v2 row_newbcast:1 row_mask:0xf bank_mask:0xf
  v_and_b32_dpp v15, v153, v3 row_newbcast:1 row_mask:0xf bank_mask:0xf
  v_and_b32_dpp v16, v153, v4 row_newbcast:1 row_mask:0xf bank_mask:0xf
  v_and_b32_dpp v17, v153, v5 row_newbcast:1 row_mask:0xf bank_mask:0xf
  v_mfma_f32_16x16x32_bf16 v[20:23], v[28:31], v[10:13], 0
  v_and_b32_dpp v10, v153, v2 row_newbcast:2 row_mask:0xf bank_mask:0xf
  v_and_b32_dpp v11, v153, v3 row_newbcast:2 row_mask:0xf bank_mask:0xf
  v_and_b32_dpp v12, v153, v4 row_newbcast:2 row_mask:0xf bank_mask:0xf
  v_and_b32_dpp v13, v153, v5 row_newbcast:2 row_mask:0xf bank_mask:0xf
  v_mfma_f32_16x16x32_bf16 v[20:23], v[32:35], v[14:17], v[20:23]
  v_and_b32 v9, 0xf0, v0
  v_lshlrev_b32 v9, 2, v9
  v_bfe_u32 v19, v0, 8, 2
  v_lshl_or_b32 v9, v19, 4, v9
  s_lshl_b32 s72, s65, 9
  s_add_u32 s72, s72, 73984
  v_add_u32 v9, s72, v9
  v_mfma_f32_16x16x32_bf16 v[20:23], v[36:39], v[10:13], v[20:23]
  s_mov_b32 exec_lo, 0xff00ff
  s_mov_b32 exec_hi, 0xff00ff
  s_nop 5
  ds_write_b128 v9, v[20:23]
  s_mov_b64 exec, -1
  v_and_b32 v12, 0xffff0000, v153
  s_mov_b32 s78, 0x70007
  s_mov_b32 s79, 0x30003
  v_cndmask_b32 v16, 0, v12, s[78:79]
  v_mul_f32 v17, v16, v16
  s_nop 0
  v_add_f32_dpp v16, v16, v16 quad_perm:[1,0,3,2] row_mask:0xf bank_mask:0xf
  v_add_f32_dpp v17, v17, v17 quad_perm:[1,0,3,2] row_mask:0xf bank_mask:0xf
  s_nop 0
  v_add_f32_dpp v16, v16, v16 quad_perm:[2,3,0,1] row_mask:0xf bank_mask:0xf
  v_add_f32_dpp v17, v17, v17 quad_perm:[2,3,0,1] row_mask:0xf bank_mask:0xf
  s_nop 1
  v_permlane16_swap_b32 v16, v17
  v_add_f32 v16, v16, v17
  v_mov_b32 v18, v16
  s_nop 1
  v_permlane32_swap_b32 v16, v18
  v_add_f32 v16, v16, v18
  s_lshl_b32 s72, s65, 3
  s_add_u32 s72, s72, 73984
  v_bfe_u32 v19, v0, 8, 1
  v_lshlrev_b32 v19, 2, v19
  v_add_u32 v19, s72, v19
  s_mov_b32 exec_lo, 0x10001
  s_mov_b32 exec_hi, 0x0
  ds_write_b32 v19, v16 offset:4096
  s_mov_b64 exec, -1
  v_and_b32_dpp v10, v154, v2 row_newbcast:0 row_mask:0xf bank_mask:0xf
  v_and_b32_dpp v11, v154, v3 row_newbcast:0 row_mask:0xf bank_mask:0xf
  v_and_b32_dpp v12, v154, v4 row_newbcast:0 row_mask:0xf bank_mask:0xf
  v_and_b32_dpp v13, v154, v5 row_newbcast:0 row_mask:0xf bank_mask:0xf
  v_and_b32_dpp v14, v154, v2 row_newbcast:1 row_mask:0xf bank_mask:0xf
  v_and_b32_dpp v15, v154, v3 row_newbcast:1 row_mask:0xf bank_mask:0xf
  v_and_b32_dpp v16, v154, v4 row_newbcast:1 row_mask:0xf bank_mask:0xf
  v_and_b32_dpp v17, v154, v5 row_newbcast:1 row_mask:0xf bank_mask:0xf
  v_mfma_f32_16x16x32_bf16 v[20:23], v[28:31], v[10:13], 0
  v_and_b32_dpp v10, v154, v2 row_newbcast:2 row_mask:0xf bank_mask:0xf
  v_and_b32_dpp v11, v154, v3 row_newbcast:2 row_mask:0xf bank_mask:0xf
  v_and_b32_dpp v12, v154, v4 row_newbcast:2 row_mask:0xf bank_mask:0xf
  v_and_b32_dpp v13, v154, v5 row_newbcast:2 row_mask:0xf bank_mask:0xf
  v_mfma_f32_16x16x32_bf16 v[20:23], v[32:35], v[14:17], v[20:23]
  v_and_b32 v9, 0xf0, v0
  v_lshlrev_b32 v9, 2, v9
  v_bfe_u32 v19, v0, 8, 2
  v_lshl_or_b32 v9, v19, 4, v9
  s_lshl_b32 s72, s65, 9
  s_add_u32 s72, s72, 78208
  v_add_u32 v9, s72, v9
  v_mfma_f32_16x16x32_bf16 v[20:23], v[36:39], v[10:13], v[20:23]
  s_mov_b32 exec_lo, 0xff00ff
  s_mov_b32 exec_hi, 0xff00ff
  s_nop 5
  ds_write_b128 v9, v[20:23]
  s_mov_b64 exec, -1
  v_and_b32 v12, 0xffff0000, v154
  s_mov_b32 s78, 0x70007
  s_mov_b32 s79, 0x30003
  v_cndmask_b32 v16, 0, v12, s[78:79]
  v_mul_f32 v17, v16, v16
  s_nop 0
  v_add_f32_dpp v16, v16, v16 quad_perm:[1,0,3,2] row_mask:0xf bank_mask:0xf
  v_add_f32_dpp v17, v17, v17 quad_perm:[1,0,3,2] row_mask:0xf bank_mask:0xf
  s_nop 0
  v_add_f32_dpp v16, v16, v16 quad_perm:[2,3,0,1] row_mask:0xf bank_mask:0xf
  v_add_f32_dpp v17, v17, v17 quad_perm:[2,3,0,1] row_mask:0xf bank_mask:0xf
  s_nop 1
  v_permlane16_swap_b32 v16, v17
  v_add_f32 v16, v16, v17
  v_mov_b32 v18, v16
  s_nop 1
  v_permlane32_swap_b32 v16, v18
  v_add_f32 v16, v16, v18
  s_lshl_b32 s72, s65, 3
  s_add_u32 s72, s72, 78208
  v_bfe_u32 v19, v0, 8, 1
  v_lshlrev_b32 v19, 2, v19
  v_add_u32 v19, s72, v19
  s_mov_b32 exec_lo, 0x10001
  s_mov_b32 exec_hi, 0x0
  ds_write_b32 v19, v16 offset:4096
  s_mov_b64 exec, -1
  v_and_b32_dpp v10, v155, v2 row_newbcast:0 row_mask:0xf bank_mask:0xf
  v_and_b32_dpp v11, v155, v3 row_newbcast:0 row_mask:0xf bank_mask:0xf
  v_and_b32_dpp v12, v155, v4 row_newbcast:0 row_mask:0xf bank_mask:0xf
  v_and_b32_dpp v13, v155, v5 row_newbcast:0 row_mask:0xf bank_mask:0xf
  v_and_b32_dpp v14, v155, v2 row_newbcast:1 row_mask:0xf bank_mask:0xf
  v_and_b32_dpp v15, v155, v3 row_newbcast:1 row_mask:0xf bank_mask:0xf
  v_and_b32_dpp v16, v155, v4 row_newbcast:1 row_mask:0xf bank_mask:0xf
  v_and_b32_dpp v17, v155, v5 row_newbcast:1 row_mask:0xf bank_mask:0xf
  v_mfma_f32_16x16x32_bf16 v[20:23], v[28:31], v[10:13], 0
  v_and_b32_dpp v10, v155, v2 row_newbcast:2 row_mask:0xf bank_mask:0xf
  v_and_b32_dpp v11, v155, v3 row_newbcast:2 row_mask:0xf bank_mask:0xf
  v_and_b32_dpp v12, v155, v4 row_newbcast:2 row_mask:0xf bank_mask:0xf
  v_and_b32_dpp v13, v155, v5 row_newbcast:2 row_mask:0xf bank_mask:0xf
  v_mfma_f32_16x16x32_bf16 v[20:23], v[32:35], v[14:17], v[20:23]
  v_and_b32 v9, 0xf0, v0
  v_lshlrev_b32 v9, 2, v9
  v_bfe_u32 v19, v0, 8, 2
  v_lshl_or_b32 v9, v19, 4, v9
  s_lshl_b32 s72, s65, 9
  s_add_u32 s72, s72, 82432
  v_add_u32 v9, s72, v9
  v_mfma_f32_16x16x32_bf16 v[20:23], v[36:39], v[10:13], v[20:23]
  s_mov_b32 exec_lo, 0xff00ff
  s_mov_b32 exec_hi, 0xff00ff
  s_nop 5
  ds_write_b128 v9, v[20:23]
  s_mov_b64 exec, -1
  v_and_b32 v12, 0xffff0000, v155
  s_mov_b32 s78, 0x70007
  s_mov_b32 s79, 0x30003
  v_cndmask_b32 v16, 0, v12, s[78:79]
  v_mul_f32 v17, v16, v16
  s_nop 0
  v_add_f32_dpp v16, v16, v16 quad_perm:[1,0,3,2] row_mask:0xf bank_mask:0xf
  v_add_f32_dpp v17, v17, v17 quad_perm:[1,0,3,2] row_mask:0xf bank_mask:0xf
  s_nop 0
  v_add_f32_dpp v16, v16, v16 quad_perm:[2,3,0,1] row_mask:0xf bank_mask:0xf
  v_add_f32_dpp v17, v17, v17 quad_perm:[2,3,0,1] row_mask:0xf bank_mask:0xf
  s_nop 1
  v_permlane16_swap_b32 v16, v17
  v_add_f32 v16, v16, v17
  v_mov_b32 v18, v16
  s_nop 1
  v_permlane32_swap_b32 v16, v18
  v_add_f32 v16, v16, v18
  s_lshl_b32 s72, s65, 3
  s_add_u32 s72, s72, 82432
  v_bfe_u32 v19, v0, 8, 1
  v_lshlrev_b32 v19, 2, v19
  v_add_u32 v19, s72, v19
  s_mov_b32 exec_lo, 0x10001
  s_mov_b32 exec_hi, 0x0
  ds_write_b32 v19, v16 offset:4096
  s_mov_b64 exec, -1
  v_and_b32_dpp v10, v159, v2 row_newbcast:0 row_mask:0xf bank_mask:0xf
  v_and_b32_dpp v11, v159, v3 row_newbcast:0 row_mask:0xf bank_mask:0xf
  v_and_b32_dpp v12, v159, v4 row_newbcast:0 row_mask:0xf bank_mask:0xf
  v_and_b32_dpp v13, v159, v5 row_newbcast:0 row_mask:0xf bank_mask:0xf
  v_and_b32_dpp v14, v159, v2 row_newbcast:1 row_mask:0xf bank_mask:0xf
  v_and_b32_dpp v15, v159, v3 row_newbcast:1 row_mask:0xf bank_mask:0xf
  v_and_b32_dpp v16, v159, v4 row_newbcast:1 row_mask:0xf bank_mask:0xf
  v_and_b32_dpp v17, v159, v5 row_newbcast:1 row_mask:0xf bank_mask:0xf
  v_mfma_f32_16x16x32_bf16 v[20:23], v[140:143], v[10:13], 0
  v_and_b32_dpp v10, v159, v2 row_newbcast:2 row_mask:0xf bank_mask:0xf
  v_and_b32_dpp v11, v159, v3 row_newbcast:2 row_mask:0xf bank_mask:0xf
  v_and_b32_dpp v12, v159, v4 row_newbcast:2 row_mask:0xf bank_mask:0xf
  v_and_b32_dpp v13, v159, v5 row_newbcast:2 row_mask:0xf bank_mask:0xf
  v_mfma_f32_16x16x32_bf16 v[20:23], v[144:147], v[14:17], v[20:23]
  v_and_b32 v9, 0xf0, v0
  v_lshlrev_b32 v9, 2, v9
  v_bfe_u32 v19, v0, 8, 2
  v_lshl_or_b32 v9, v19, 4, v9
  s_lshl_b32 s72, s65, 9
  s_add_u32 s72, s72, 86656
  v_add_u32 v9, s72, v9
  v_mfma_f32_16x16x32_bf16 v[20:23], v[148:151], v[10:13], v[20:23]
  s_mov_b32 exec_lo, 0xff00ff
  s_mov_b32 exec_hi, 0xff00ff
  s_nop 5
  ds_write_b128 v9, v[20:23]
)ASM"
R"ASM(
  s_mov_b64 exec, -1
  s_waitcnt lgkmcnt(0)
  s_barrier
  s_cmp_gt_u32 s65, 5
  s_cbranch_scc1 .p2p10
  s_add_u32 s72, s65, 15
  s_cmp_eq_u32 s65, 0
  s_cselect_b32 s72, 20, s72
  s_cmp_eq_u32 s65, 5
  s_cselect_b32 s72, 21, s72
  s_add_u32 s62, s60, s72
  s_add_u32 s62, s62, 1
  s_or_b32 s62, s62, 0x80000000
  s_sub_u32 s72, s72, 12
  s_mul_i32 s95, s72, 589824
  s_mul_i32 s76, s65, 4224
  s_add_u32 s76, s76, 65536
  s_setprio 3
  v_bfe_u32 v8, v0, 4, 6
  v_lshlrev_b32 v6, 3, v8
  v_add_u32 v6, s76, v6
  v_and_b32 v7, 7, v8
  v_lshlrev_b32 v7, 3, v7
  v_add_u32 v7, s76, v7
  v_lshlrev_b32 v8, 4, v8
  ds_read_b64 v[12:13], v6 offset:0
  ds_read_b64 v[14:15], v6 offset:512
  ds_read_b64 v[16:17], v6 offset:1024
  ds_read_b64 v[18:19], v6 offset:1536
  ds_read_b64 v[20:21], v6 offset:2048
  ds_read_b64 v[22:23], v6 offset:2560
  ds_read_b64 v[24:25], v6 offset:3072
  ds_read_b64 v[26:27], v6 offset:3584
  ds_read_b64 v[28:29], v7 offset:4096
  s_add_u32 s72, s95, s98
  s_waitcnt lgkmcnt(5)
  v_add_f32 v12, v12, v14
  v_add_f32 v13, v13, v15
  v_add_f32 v16, v16, v18
  v_add_f32 v17, v17, v19
  s_waitcnt lgkmcnt(1)
  v_add_f32 v20, v20, v22
  v_add_f32 v21, v21, v23
  v_add_f32 v24, v24, v26
  v_add_f32 v25, v25, v27
  v_add_f32 v12, v12, v16
  v_add_f32 v13, v13, v17
  v_add_f32 v20, v20, v24
  v_add_f32 v21, v21, v25
  v_add_f32 v12, v12, v20
  v_add_f32 v13, v13, v21
  s_waitcnt lgkmcnt(0)
  v_add_f32_dpp v28, v28, v28 quad_perm:[1,0,3,2] row_mask:0xf bank_mask:0xf
  v_add_f32_dpp v29, v29, v29 quad_perm:[1,0,3,2] row_mask:0xf bank_mask:0xf
  s_nop 0
  v_add_f32_dpp v28, v28, v28 quad_perm:[2,3,0,1] row_mask:0xf bank_mask:0xf
  v_add_f32_dpp v29, v29, v29 quad_perm:[2,3,0,1] row_mask:0xf bank_mask:0xf
  s_nop 0
  v_add_f32_dpp v28, v28, v28 row_half_mirror row_mask:0xf bank_mask:0xf
  v_add_f32_dpp v29, v29, v29 row_half_mirror row_mask:0xf bank_mask:0xf
  v_cndmask_b32 v14, v28, v29, s[80:81]
  v_mov_b32 v15, s62
  buffer_store_dwordx4 v[12:15], v8, s[48:51], s72 offen sc1
  s_setprio 0
  s_nop 0
.ub11:
.p2p10:
  s_add_u32 s75, s68, 98304
  buffer_load_dwordx4 v[56:59], v1, s[16:19], s75 offen nt
  s_add_u32 s75, s68, 106496
  buffer_load_dwordx4 v[60:63], v1, s[16:19], s75 offen nt
  s_add_u32 s75, s68, 114688
  buffer_load_dwordx4 v[64:67], v1, s[16:19], s75 offen nt
  s_add_u32 s75, s68, 122880
  buffer_load_dwordx4 v[68:71], v1, s[16:19], s75 offen nt
  s_add_u32 s75, s68, 0
  buffer_load_dwordx4 v[72:75], v0, s[20:23], s75 offen nt
  s_add_u32 s75, s68, 8192
  buffer_load_dwordx4 v[76:79], v0, s[20:23], s75 offen nt
  s_branch .c29
.c18:
  s_lshl_b32 s72, s64, 2
  s_add_u32 s72, s72, 62
  s_sub_u32 s72, s72, 64
  s_add_u32 s62, s60, s72
  s_lshr_b32 s95, s72, 1
  s_and_b32 s95, s95, 2
  s_or_b32 s95, s95, 0
  s_mul_i32 s95, s95, 589824
  s_add_u32 s62, s62, 1
  s_or_b32 s62, s62, 0x80000000
  v_bfe_u32 v19, v0, 8, 1
  v_mul_u32_u24 v19, 10240, v19
  v_bfe_u32 v18, v0, 9, 1
  v_mul_u32_u24 v18, 81920, v18
  v_add_u32 v19, v19, v18
  v_and_b32 v18, 0xf0, v0
  v_add_u32 v19, v19, v18
  s_mul_i32 s73, s65, 1280
  s_mov_b32 exec_lo, 0x3ff03ff
  s_mov_b32 exec_hi, 0x3ff03ff
  s_add_u32 s74, s73, 0
  buffer_load_dwordx4 v[24:27], v19, s[32:35], s74 offen
  s_add_u32 s74, s73, 20480
  buffer_load_dwordx4 v[28:31], v19, s[32:35], s74 offen
  s_add_u32 s74, s73, 40960
  buffer_load_dwordx4 v[32:35], v19, s[32:35], s74 offen
  s_add_u32 s74, s73, 61440
  buffer_load_dwordx4 v[36:39], v19, s[32:35], s74 offen
  s_mov_b64 exec, -1
  v_bfe_u32 v8, v0, 4, 6
  v_mul_u32_u24 v6, 52, v8
  v_lshrrev_b32 v6, 8, v6
  v_mul_u32_u24 v7, 5, v6
  v_sub_u32 v7, v8, v7
  v_lshlrev_b32 v6, 6, v6
  v_lshl_add_u32 v6, v7, 3, v6
  v_add_u32 v6, s70, v6
  v_mul_u32_u24 v7, 12, v8
  v_lshlrev_b32 v8, 4, v8
  v_bfe_u32 v9, v0, 4, 6
  v_lshrrev_b32 v20, 3, v9
  v_mul_u32_u24 v20, 1024, v20
  v_and_b32 v9, 7, v9
  v_lshl_add_u32 v20, v9, 4, v20
  s_mul_i32 s76, s66, 8192
  s_add_u32 s76, s76, s95
  s_lshl_b32 s73, s65, 7
  s_add_u32 s76, s76, s73
  s_sleep 20
  buffer_load_dwordx4 v[10:13], v20, s[48:51], s76 offen sc1
  s_waitcnt vmcnt(0)
  v_cmp_eq_u32 vcc, s62, v13
  s_nop 0
  s_mov_b64 s[78:79], vcc
  s_cmp_eq_u64 s[78:79], exec
  s_nop 1
  s_cbranch_scc1 .ok12
  s_mov_b32 s94, 0
.re13:
  s_cmp_lg_u32 s71, 0
  s_cbranch_scc1 .ok12
  s_add_u32 s94, s94, 1
  s_cmp_gt_u32 s94, 200000
  s_cselect_b32 s71, 1, 0
  buffer_load_dwordx4 v[10:13], v20, s[48:51], s76 offen sc1
  s_waitcnt vmcnt(0)
  v_cmp_eq_u32 vcc, s62, v13
  s_nop 0
  s_mov_b64 s[78:79], vcc
  s_cmp_eq_u64 s[78:79], exec
  s_nop 1
  s_cbranch_scc0 .re13
.ok12:
  s_add_u32 s75, s68, 98304
  buffer_load_dwordx4 v[56:59], v1, s[16:19], s75 offen nt
  s_add_u32 s75, s68, 106496
  buffer_load_dwordx4 v[60:63], v1, s[16:19], s75 offen nt
  s_add_u32 s75, s68, 114688
  buffer_load_dwordx4 v[64:67], v1, s[16:19], s75 offen nt
  v_mov_b32_dpp v14, v12 quad_perm:[0,0,2,2] row_mask:0xf bank_mask:0xf
  v_mov_b32_dpp v15, v12 quad_perm:[1,1,3,3] row_mask:0xf bank_mask:0xf
  v_mov_b32 v20, v14
  s_nop 1
  v_max_f32_dpp v20, v20, v20 row_ror:8 row_mask:0xf bank_mask:0xf
  v_mov_b32 v21, v20
  s_nop 1
  v_permlane16_swap_b32 v20, v21
  v_max_f32 v20, v20, v21
  v_mov_b32 v21, v20
  s_nop 1
  v_permlane32_swap_b32 v20, v21
  v_max_f32 v20, v20, v21
  v_sub_f32 v21, v14, v20
  v_exp_f32 v21, v21
  s_nop 0
  v_mul_f32 v22, v21, v15
  v_mul_f32 v10, v10, v21
  v_mul_f32 v11, v11, v21
  v_add_f32_dpp v22, v22, v22 row_ror:8 row_mask:0xf bank_mask:0xf
  v_add_f32_dpp v10, v10, v10 row_ror:8 row_mask:0xf bank_mask:0xf
  v_add_f32_dpp v11, v11, v11 row_ror:8 row_mask:0xf bank_mask:0xf
  v_mov_b32 v23, v22
  s_nop 1
  v_permlane16_swap_b32 v22, v23
  v_permlane16_swap_b32 v10, v11
  v_add_f32 v22, v22, v23
  v_add_f32 v10, v10, v11
  v_mov_b32 v23, v22
  v_mov_b32 v12, v10
  s_nop 0
  v_permlane32_swap_b32 v22, v23
  v_permlane32_swap_b32 v10, v12
  v_add_f32 v22, v22, v23
  v_add_f32 v10, v10, v12
  v_rcp_f32 v22, v22
  s_nop 0
  v_mov_b32_dpp v10, v10 row_shl:4 row_mask:0xc bank_mask:0xf
  v_mul_f32 v10, v10, v22
  v_cvt_pk_bf16_f32 v18, v10, v10
)ASM"
R"ASM(
  s_nop 1
  v_and_b32_dpp v10, v18, v2 row_newbcast:0 row_mask:0xf bank_mask:0xf
  v_and_b32_dpp v11, v18, v3 row_newbcast:0 row_mask:0xf bank_mask:0xf
  v_and_b32_dpp v12, v18, v4 row_newbcast:0 row_mask:0xf bank_mask:0xf
  v_and_b32_dpp v13, v18, v5 row_newbcast:0 row_mask:0xf bank_mask:0xf
  v_and_b32_dpp v14, v18, v2 row_newbcast:1 row_mask:0xf bank_mask:0xf
  v_and_b32_dpp v15, v18, v3 row_newbcast:1 row_mask:0xf bank_mask:0xf
  v_and_b32_dpp v16, v18, v4 row_newbcast:1 row_mask:0xf bank_mask:0xf
  v_and_b32_dpp v17, v18, v5 row_newbcast:1 row_mask:0xf bank_mask:0xf
  v_mfma_f32_16x16x32_bf16 v[20:23], v[24:27], v[10:13], 0
  v_and_b32_dpp v10, v18, v2 row_newbcast:2 row_mask:0xf bank_mask:0xf
  v_and_b32_dpp v11, v18, v3 row_newbcast:2 row_mask:0xf bank_mask:0xf
  v_and_b32_dpp v12, v18, v4 row_newbcast:2 row_mask:0xf bank_mask:0xf
  v_and_b32_dpp v13, v18, v5 row_newbcast:2 row_mask:0xf bank_mask:0xf
  v_mfma_f32_16x16x32_bf16 v[20:23], v[28:31], v[14:17], v[20:23]
  v_and_b32_dpp v14, v18, v2 row_newbcast:3 row_mask:0xf bank_mask:0xf
  v_and_b32_dpp v15, v18, v3 row_newbcast:3 row_mask:0xf bank_mask:0xf
  v_and_b32_dpp v16, v18, v4 row_newbcast:3 row_mask:0xf bank_mask:0xf
  v_and_b32_dpp v17, v18, v5 row_newbcast:3 row_mask:0xf bank_mask:0xf
  v_mfma_f32_16x16x32_bf16 v[20:23], v[32:35], v[10:13], v[20:23]
  v_and_b32 v9, 0xf0, v0
  v_lshlrev_b32 v9, 2, v9
  v_bfe_u32 v19, v0, 8, 2
  v_lshl_or_b32 v9, v19, 4, v9
  s_lshl_b32 s72, s65, 9
  s_add_u32 s72, s72, s70
  v_add_u32 v9, s72, v9
  v_mfma_f32_16x16x32_bf16 v[20:23], v[36:39], v[14:17], v[20:23]
  s_mov_b32 exec_lo, 0xff00ff
  s_mov_b32 exec_hi, 0xff00ff
  s_nop 5
  ds_write_b128 v9, v[20:23]
  s_mov_b64 exec, -1
  s_add_u32 s75, s68, 122880
  buffer_load_dwordx4 v[68:71], v1, s[16:19], s75 offen nt
  s_add_u32 s75, s68, 0
  buffer_load_dwordx4 v[72:75], v0, s[20:23], s75 offen nt
  s_add_u32 s75, s68, 8192
  buffer_load_dwordx4 v[76:79], v0, s[20:23], s75 offen nt
  s_add_u32 s72, s95, s97
  s_waitcnt lgkmcnt(0)
  s_barrier
  s_cmp_lg_u32 s65, 1
  s_cbranch_scc1 .ya14
  s_setprio 3
  s_mov_b32 exec_lo, 0xffffffff
  s_mov_b32 exec_hi, 0xff
  ds_read_b64 v[12:13], v6 offset:0
  ds_read_b64 v[14:15], v6 offset:512
  ds_read_b64 v[16:17], v6 offset:1024
  ds_read_b64 v[18:19], v6 offset:1536
  ds_read_b64 v[20:21], v6 offset:2048
  ds_read_b64 v[22:23], v6 offset:2560
  ds_read_b64 v[24:25], v6 offset:3072
  ds_read_b64 v[26:27], v6 offset:3584
  s_mov_b32 s78, 0xffffffff
  s_mov_b32 s79, 0xff
  s_waitcnt lgkmcnt(4)
  v_add_f32 v12, v12, v14
  v_add_f32 v13, v13, v15
  v_add_f32 v16, v16, v18
  v_add_f32 v17, v17, v19
  s_waitcnt lgkmcnt(0)
  v_add_f32 v20, v20, v22
  v_add_f32 v21, v21, v23
  v_add_f32 v24, v24, v26
  v_add_f32 v25, v25, v27
  v_add_f32 v12, v12, v16
  v_add_f32 v13, v13, v17
  v_add_f32 v20, v20, v24
  v_add_f32 v21, v21, v25
  v_add_f32 v12, v12, v20
  v_add_f32 v13, v13, v21
  v_cvt_pk_bf16_f32 v16, v12, v13
  s_mov_b64 exec, -1
  v_cndmask_b32 v16, 0, v16, s[78:79]
  ds_bpermute_b32 v12, v7, v16
  ds_bpermute_b32 v13, v7, v16 offset:4
  ds_bpermute_b32 v14, v7, v16 offset:8
  v_mov_b32 v15, s62
  s_waitcnt lgkmcnt(0)
  s_mov_b32 exec_lo, 0xffff
  s_mov_b32 exec_hi, 0x0
  buffer_store_dwordx4 v[12:15], v8, s[48:51], s72 offen sc1
  s_mov_b64 exec, -1
  s_setprio 0
.ya14:
  s_xor_b32 s70, s70, 4160
.c29:
  s_cmp_eq_u32 s64, 0
  s_cbranch_scc0 .c118
  s_add_u32 s75, s68, 16384
  buffer_load_dwordx4 v[80:83], v0, s[20:23], s75 offen nt
  s_add_u32 s75, s68, 24576
  buffer_load_dwordx4 v[84:87], v0, s[20:23], s75 offen nt
  s_add_u32 s75, s68, 32768
  buffer_load_dwordx4 v[88:91], v0, s[20:23], s75 offen nt
  s_add_u32 s75, s68, 40960
  buffer_load_dwordx4 v[92:95], v0, s[20:23], s75 offen nt
  s_add_u32 s75, s68, 49152
  buffer_load_dwordx4 v[96:99], v0, s[20:23], s75 offen nt
  s_add_u32 s75, s68, 57344
  buffer_load_dwordx4 v[100:103], v0, s[20:23], s75 offen nt
  s_branch .c219
.c118:
  s_lshl_b32 s72, s64, 2
  s_add_u32 s72, s72, 62
  s_sub_u32 s72, s72, 64
  s_add_u32 s62, s60, s72
  s_lshr_b32 s95, s72, 1
  s_and_b32 s95, s95, 2
  s_or_b32 s95, s95, 0
  s_mul_i32 s95, s95, 589824
  s_add_u32 s62, s62, 1
  s_or_b32 s62, s62, 0x80000000
  v_bfe_u32 v22, v0, 9, 1
  v_mul_u32_u24 v22, 393216, v22
  v_bfe_u32 v23, v0, 8, 1
  v_lshl_add_u32 v22, v23, 16, v22
  v_and_b32 v23, 0xf0, v0
  v_add_u32 v22, v22, v23
  s_lshl_b32 s73, s65, 13
  s_add_u32 s74, s73, 0
  buffer_load_dwordx4 v[28:31], v22, s[44:47], s74 offen
  s_add_u32 s74, s73, 131072
  buffer_load_dwordx4 v[32:35], v22, s[44:47], s74 offen
  s_add_u32 s74, s73, 262144
  buffer_load_dwordx4 v[36:39], v22, s[44:47], s74 offen
  s_xor_b32 s72, s70, 4160
  v_bfe_u32 v8, v0, 4, 6
  v_lshlrev_b32 v6, 3, v8
  v_add_u32 v6, s72, v6
  v_and_b32 v7, 7, v8
  v_lshlrev_b32 v7, 3, v7
  v_add_u32 v7, s72, v7
  v_lshlrev_b32 v8, 4, v8
  v_bfe_u32 v9, v0, 4, 6
  v_lshlrev_b32 v23, 4, v9
  s_lshl_b32 s76, s67, 5
  s_lshl_b32 s77, s65, 2
  s_add_u32 s76, s76, s77
  s_mul_i32 s76, s76, 256
  s_add_u32 s76, s76, s95
  s_add_u32 s76, s76, 262144
  v_bfe_u32 v9, v0, 4, 4
  v_bfe_u32 v27, v0, 9, 1
  v_mul_u32_u24 v27, 3, v27
  v_add_u32 v9, v9, v27
  v_bfe_u32 v27, v0, 8, 1
  v_lshl_or_b32 v9, v9, 1, v27
  s_add_u32 s77, s64, 1
  s_and_b32 s77, s77, 1
  s_lshl_b32 s77, s77, 10
  s_add_u32 s77, s77, 158144
  s_lshl_b32 s72, s65, 6
  s_add_u32 s77, s77, s72
  v_lshlrev_b32 v27, 2, v9
  v_add_u32 v27, s77, v27
  ds_read_b32 v22, v27
  v_bfe_u32 v25, v0, 4, 4
  v_mul_u32_u24 v25, 24, v25
  v_bfe_u32 v26, v0, 8, 1
  v_lshl_add_u32 v25, v26, 2, v25
  s_mul_i32 s72, s65, 384
  s_add_u32 s72, s72, s70
  v_add_u32 v25, s72, v25
  s_mul_i32 s72, s65, 40
  s_add_u32 s72, s72, s70
  v_lshlrev_b32 v26, 2, v9
  v_add_u32 v26, s72, v26
  s_sleep 30
  buffer_load_dwordx4 v[10:13], v23, s[48:51], s76 offen sc1
  s_waitcnt vmcnt(0)
  v_cmp_eq_u32 vcc, s62, v13
  s_nop 0
  s_mov_b64 s[78:79], vcc
  s_cmp_eq_u64 s[78:79], exec
  s_nop 1
  s_cbranch_scc1 .ok20
  s_mov_b32 s94, 0
.re21:
  s_cmp_lg_u32 s71, 0
  s_cbranch_scc1 .ok20
  s_add_u32 s94, s94, 1
  s_cmp_gt_u32 s94, 200000
  s_cselect_b32 s71, 1, 0
  buffer_load_dwordx4 v[10:13], v23, s[48:51], s76 offen sc1
  s_waitcnt vmcnt(0)
  v_cmp_eq_u32 vcc, s62, v13
  s_nop 0
  s_mov_b64 s[78:79], vcc
  s_cmp_eq_u64 s[78:79], exec
  s_nop 1
  s_cbranch_scc0 .re21
.ok20:
  s_add_u32 s75, s68, 16384
  buffer_load_dwordx4 v[80:83], v0, s[20:23], s75 offen nt
  s_add_u32 s75, s68, 24576
  buffer_load_dwordx4 v[84:87], v0, s[20:23], s75 offen nt
  s_add_u32 s75, s68, 32768
)ASM"
R"ASM(
  buffer_load_dwordx4 v[88:91], v0, s[20:23], s75 offen nt
  v_lshlrev_b32 v14, 16, v10
  v_and_b32 v15, 0xffff0000, v10
  v_lshlrev_b32 v16, 16, v11
  v_and_b32 v17, 0xffff0000, v11
  v_lshlrev_b32 v18, 16, v12
  v_and_b32 v19, 0xffff0000, v12
  v_permlane16_swap_b32 v14, v15
  v_permlane16_swap_b32 v16, v17
  v_permlane16_swap_b32 v18, v19
  v_add_f32 v14, v14, v15
  v_add_f32 v16, v16, v17
  v_add_f32 v18, v18, v19
  v_mov_b32 v20, v14
  v_mov_b32 v21, v16
  v_mov_b32 v24, v18
  v_permlane32_swap_b32 v14, v20
  v_permlane32_swap_b32 v16, v21
  v_permlane32_swap_b32 v18, v24
  v_add_f32 v14, v14, v20
  v_add_f32 v16, v16, v21
  v_add_f32 v18, v18, v24
  s_mov_b32 exec_lo, 0x3fff3fff
  s_mov_b32 exec_hi, 0x0
  ds_write_b32 v25, v14
  ds_write_b32 v25, v16 offset:8
  ds_write_b32 v25, v18 offset:16
  s_mov_b64 exec, -1
  s_waitcnt lgkmcnt(0)
  s_barrier
  ds_read_b32 v12, v26 offset:0
  ds_read_b32 v13, v26 offset:384
  ds_read_b32 v14, v26 offset:768
  ds_read_b32 v15, v26 offset:1152
  ds_read_b32 v16, v26 offset:1536
  ds_read_b32 v17, v26 offset:1920
  ds_read_b32 v18, v26 offset:2304
  ds_read_b32 v19, v26 offset:2688
  s_waitcnt lgkmcnt(4)
  v_add_f32 v12, v12, v13
  v_add_f32 v14, v14, v15
  s_waitcnt lgkmcnt(0)
  v_add_f32 v16, v16, v17
  v_add_f32 v18, v18, v19
  v_add_f32 v12, v12, v14
  v_add_f32 v16, v16, v18
  v_add_f32 v10, v12, v16
  v_cvt_pk_bf16_f32 v10, v10, v10
  s_nop 0
  v_and_b32 v10, 0xffff0000, v10
  v_add_f32 v12, v22, v10
  v_cvt_pk_bf16_f32 v13, v12, v12
  s_nop 0
  v_and_b32 v12, 0xffff0000, v13
  s_xor_b32 s70, s70, 4160
  v_cmp_gt_u32 vcc, 10, v9
  v_mov_b32 v27, v12
  s_nop 0
  v_cndmask_b32 v24, 0, v13, vcc
  s_nop 1
  v_and_b32_dpp v10, v24, v2 row_newbcast:0 row_mask:0xf bank_mask:0xf
  v_and_b32_dpp v11, v24, v3 row_newbcast:0 row_mask:0xf bank_mask:0xf
  v_and_b32_dpp v12, v24, v4 row_newbcast:0 row_mask:0xf bank_mask:0xf
  v_and_b32_dpp v13, v24, v5 row_newbcast:0 row_mask:0xf bank_mask:0xf
  v_and_b32_dpp v14, v24, v2 row_newbcast:1 row_mask:0xf bank_mask:0xf
  v_and_b32_dpp v15, v24, v3 row_newbcast:1 row_mask:0xf bank_mask:0xf
  v_and_b32_dpp v16, v24, v4 row_newbcast:1 row_mask:0xf bank_mask:0xf
  v_and_b32_dpp v17, v24, v5 row_newbcast:1 row_mask:0xf bank_mask:0xf
  v_mfma_f32_16x16x32_bf16 v[20:23], v[28:31], v[10:13], 0
  v_and_b32_dpp v10, v24, v2 row_newbcast:2 row_mask:0xf bank_mask:0xf
  v_and_b32_dpp v11, v24, v3 row_newbcast:2 row_mask:0xf bank_mask:0xf
  v_and_b32_dpp v12, v24, v4 row_newbcast:2 row_mask:0xf bank_mask:0xf
  v_and_b32_dpp v13, v24, v5 row_newbcast:2 row_mask:0xf bank_mask:0xf
  v_mfma_f32_16x16x32_bf16 v[20:23], v[32:35], v[14:17], v[20:23]
  s_add_u32 s77, s64, 1
  s_and_b32 s77, s77, 1
  s_lshl_b32 s77, s77, 10
  s_add_u32 s77, s77, 158656
  s_lshl_b32 s72, s65, 6
  s_add_u32 s77, s77, s72
  v_lshlrev_b32 v9, 2, v9
  v_add_u32 v9, s77, v9
  v_mfma_f32_16x16x32_bf16 v[20:23], v[36:39], v[10:13], v[20:23]
  s_mov_b32 exec_lo, 0x70007
  s_mov_b32 exec_hi, 0x30003
  ds_write_b32 v9, v27
  s_mov_b64 exec, -1
  v_and_b32 v9, 0xf0, v0
  v_lshlrev_b32 v9, 2, v9
  v_bfe_u32 v25, v0, 8, 2
  v_lshl_or_b32 v9, v25, 4, v9
  s_lshl_b32 s72, s65, 9
  s_add_u32 s72, s72, s70
  v_add_u32 v9, s72, v9
  s_mov_b32 s78, 0x70007
  s_mov_b32 s79, 0x30003
  v_cndmask_b32 v16, 0, v27, s[78:79]
  v_mul_f32 v17, v16, v16
  s_nop 0
  v_add_f32_dpp v16, v16, v16 quad_perm:[1,0,3,2] row_mask:0xf bank_mask:0xf
  v_add_f32_dpp v17, v17, v17 quad_perm:[1,0,3,2] row_mask:0xf bank_mask:0xf
  s_nop 0
  v_add_f32_dpp v16, v16, v16 quad_perm:[2,3,0,1] row_mask:0xf bank_mask:0xf
  v_add_f32_dpp v17, v17, v17 quad_perm:[2,3,0,1] row_mask:0xf bank_mask:0xf
  s_nop 1
  v_permlane16_swap_b32 v16, v17
  v_add_f32 v16, v16, v17
  v_mov_b32 v18, v16
  s_nop 1
  v_permlane32_swap_b32 v16, v18
  v_add_f32 v16, v16, v18
  s_lshl_b32 s72, s65, 3
  s_add_u32 s72, s72, s70
  v_bfe_u32 v19, v0, 8, 1
  v_lshlrev_b32 v19, 2, v19
  v_add_u32 v19, s72, v19
  s_mov_b32 exec_lo, 0x10001
  s_mov_b32 exec_hi, 0x0
  ds_write_b32 v19, v16 offset:4096
  s_mov_b64 exec, -1
  s_mov_b32 exec_lo, 0xff00ff
  s_mov_b32 exec_hi, 0xff00ff
  ds_write_b128 v9, v[20:23]
  s_mov_b64 exec, -1
  s_add_u32 s75, s68, 40960
  buffer_load_dwordx4 v[92:95], v0, s[20:23], s75 offen nt
  s_add_u32 s75, s68, 49152
  buffer_load_dwordx4 v[96:99], v0, s[20:23], s75 offen nt
  s_add_u32 s75, s68, 57344
  buffer_load_dwordx4 v[100:103], v0, s[20:23], s75 offen nt
  s_waitcnt lgkmcnt(0)
  s_barrier
  s_cmp_lg_u32 s65, 2
  s_cbranch_scc1 .ub22
  s_setprio 3
  ds_read_b64 v[12:13], v6 offset:0
  ds_read_b64 v[14:15], v6 offset:512
  ds_read_b64 v[16:17], v6 offset:1024
  ds_read_b64 v[18:19], v6 offset:1536
  ds_read_b64 v[20:21], v6 offset:2048
  ds_read_b64 v[22:23], v6 offset:2560
  ds_read_b64 v[24:25], v6 offset:3072
  ds_read_b64 v[26:27], v6 offset:3584
  ds_read_b64 v[28:29], v7 offset:4096
  s_add_u32 s72, s95, s98
  s_waitcnt lgkmcnt(5)
  v_add_f32 v12, v12, v14
  v_add_f32 v13, v13, v15
  v_add_f32 v16, v16, v18
  v_add_f32 v17, v17, v19
  s_waitcnt lgkmcnt(1)
  v_add_f32 v20, v20, v22
  v_add_f32 v21, v21, v23
  v_add_f32 v24, v24, v26
  v_add_f32 v25, v25, v27
  v_add_f32 v12, v12, v16
  v_add_f32 v13, v13, v17
  v_add_f32 v20, v20, v24
  v_add_f32 v21, v21, v25
  v_add_f32 v12, v12, v20
  v_add_f32 v13, v13, v21
  s_waitcnt lgkmcnt(0)
  v_add_f32_dpp v28, v28, v28 quad_perm:[1,0,3,2] row_mask:0xf bank_mask:0xf
  v_add_f32_dpp v29, v29, v29 quad_perm:[1,0,3,2] row_mask:0xf bank_mask:0xf
  s_nop 0
  v_add_f32_dpp v28, v28, v28 quad_perm:[2,3,0,1] row_mask:0xf bank_mask:0xf
  v_add_f32_dpp v29, v29, v29 quad_perm:[2,3,0,1] row_mask:0xf bank_mask:0xf
  s_nop 0
  v_add_f32_dpp v28, v28, v28 row_half_mirror row_mask:0xf bank_mask:0xf
  v_add_f32_dpp v29, v29, v29 row_half_mirror row_mask:0xf bank_mask:0xf
  v_cndmask_b32 v14, v28, v29, s[80:81]
  v_mov_b32 v15, s62
  buffer_store_dwordx4 v[12:15], v8, s[48:51], s72 offen sc1
  s_setprio 0
  s_nop 0
.ub22:
  s_xor_b32 s70, s70, 4160
.c219:
  s_cmp_eq_u32 s64, 0
  s_cbranch_scc0 .c126
  v_bfe_u32 v9, v0, 4, 6
  v_lshrrev_b32 v20, 3, v9
  v_mul_u32_u24 v20, 1024, v20
  v_and_b32 v9, 7, v9
  v_lshl_add_u32 v20, v9, 4, v20
  s_mul_i32 s76, s66, 8192
  s_add_u32 s76, s76, 327680
  s_lshl_b32 s73, s65, 7
  s_add_u32 s77, s76, s73
  s_add_u32 s73, s77, 4718592
  buffer_load_dwordx4 v[136:139], v20, s[48:51], s73 offen sc1
  s_add_u32 s73, s77, 5308416
  buffer_load_dwordx4 v[140:143], v20, s[48:51], s73 offen sc1
  s_add_u32 s73, s77, 2359296
  buffer_load_dwordx4 v[144:147], v20, s[48:51], s73 offen sc1
  s_add_u32 s73, s77, 2949120
  buffer_load_dwordx4 v[148:151], v20, s[48:51], s73 offen sc1
  s_add_u32 s73, s77, 3538944
  buffer_load_dwordx4 v[152:155], v20, s[48:51], s73 offen sc1
  s_add_u32 s73, s77, 4128768
)ASM"
R"ASM(
  buffer_load_dwordx4 v[156:159], v20, s[48:51], s73 offen sc1
  s_waitcnt vmcnt(0)
  s_add_u32 s62, s60, 21
  s_or_b32 s62, s62, 0x80000000
  v_cmp_eq_u32 vcc, s62, v139
  s_nop 0
  s_mov_b64 s[78:79], vcc
  s_add_u32 s62, s60, 22
  s_or_b32 s62, s62, 0x80000000
  v_cmp_eq_u32 vcc, s62, v143
  s_nop 0
  s_and_b64 s[78:79], s[78:79], vcc
  s_add_u32 s62, s60, 17
  s_or_b32 s62, s62, 0x80000000
  v_cmp_eq_u32 vcc, s62, v147
  s_nop 0
  s_and_b64 s[78:79], s[78:79], vcc
  s_add_u32 s62, s60, 18
  s_or_b32 s62, s62, 0x80000000
  v_cmp_eq_u32 vcc, s62, v151
  s_nop 0
  s_and_b64 s[78:79], s[78:79], vcc
  s_add_u32 s62, s60, 19
  s_or_b32 s62, s62, 0x80000000
  v_cmp_eq_u32 vcc, s62, v155
  s_nop 0
  s_and_b64 s[78:79], s[78:79], vcc
  s_add_u32 s62, s60, 20
  s_or_b32 s62, s62, 0x80000000
  v_cmp_eq_u32 vcc, s62, v159
  s_nop 0
  s_and_b64 s[78:79], s[78:79], vcc
  s_cmp_eq_u64 s[78:79], exec
  s_nop 1
  s_cbranch_scc1 .ok28
  s_mov_b32 s94, 0
.re29:
  s_cmp_lg_u32 s71, 0
  s_cbranch_scc1 .ok28
  s_add_u32 s94, s94, 1
  s_cmp_gt_u32 s94, 200000
  s_cselect_b32 s71, 1, 0
  s_sleep 4
  s_add_u32 s73, s77, 4718592
  buffer_load_dwordx4 v[136:139], v20, s[48:51], s73 offen sc1
  s_add_u32 s73, s77, 5308416
  buffer_load_dwordx4 v[140:143], v20, s[48:51], s73 offen sc1
  s_add_u32 s73, s77, 2359296
  buffer_load_dwordx4 v[144:147], v20, s[48:51], s73 offen sc1
  s_add_u32 s73, s77, 2949120
  buffer_load_dwordx4 v[148:151], v20, s[48:51], s73 offen sc1
  s_add_u32 s73, s77, 3538944
  buffer_load_dwordx4 v[152:155], v20, s[48:51], s73 offen sc1
  s_add_u32 s73, s77, 4128768
  buffer_load_dwordx4 v[156:159], v20, s[48:51], s73 offen sc1
  s_waitcnt vmcnt(0)
  s_add_u32 s62, s60, 21
  s_or_b32 s62, s62, 0x80000000
  v_cmp_eq_u32 vcc, s62, v139
  s_nop 0
  s_mov_b64 s[78:79], vcc
  s_add_u32 s62, s60, 22
  s_or_b32 s62, s62, 0x80000000
  v_cmp_eq_u32 vcc, s62, v143
  s_nop 0
  s_and_b64 s[78:79], s[78:79], vcc
  s_add_u32 s62, s60, 17
  s_or_b32 s62, s62, 0x80000000
  v_cmp_eq_u32 vcc, s62, v147
  s_nop 0
  s_and_b64 s[78:79], s[78:79], vcc
  s_add_u32 s62, s60, 18
  s_or_b32 s62, s62, 0x80000000
  v_cmp_eq_u32 vcc, s62, v151
  s_nop 0
  s_and_b64 s[78:79], s[78:79], vcc
  s_add_u32 s62, s60, 19
  s_or_b32 s62, s62, 0x80000000
  v_cmp_eq_u32 vcc, s62, v155
  s_nop 0
  s_and_b64 s[78:79], s[78:79], vcc
  s_add_u32 s62, s60, 20
  s_or_b32 s62, s62, 0x80000000
  v_cmp_eq_u32 vcc, s62, v159
  s_nop 0
  s_and_b64 s[78:79], s[78:79], vcc
  s_cmp_eq_u64 s[78:79], exec
  s_nop 1
  s_cbranch_scc0 .re29
.ok28:
  s_add_u32 s75, s68, 65536
  buffer_load_dwordx4 v[104:107], v0, s[20:23], s75 offen nt
  s_add_u32 s75, s68, 73728
  buffer_load_dwordx4 v[108:111], v0, s[20:23], s75 offen nt
  s_add_u32 s75, s68, 81920
  buffer_load_dwordx4 v[112:115], v0, s[20:23], s75 offen nt
  v_add_f32_dpp v136, v136, v136 row_ror:8 row_mask:0xf bank_mask:0xf
  v_add_f32_dpp v137, v137, v137 row_ror:8 row_mask:0xf bank_mask:0xf
  v_add_f32_dpp v140, v140, v140 row_ror:8 row_mask:0xf bank_mask:0xf
  v_add_f32_dpp v141, v141, v141 row_ror:8 row_mask:0xf bank_mask:0xf
  v_add_f32_dpp v144, v144, v144 row_ror:8 row_mask:0xf bank_mask:0xf
  v_add_f32_dpp v145, v145, v145 row_ror:8 row_mask:0xf bank_mask:0xf
  v_add_f32_dpp v148, v148, v148 row_ror:8 row_mask:0xf bank_mask:0xf
  v_add_f32_dpp v149, v149, v149 row_ror:8 row_mask:0xf bank_mask:0xf
  v_add_f32_dpp v152, v152, v152 row_ror:8 row_mask:0xf bank_mask:0xf
  v_add_f32_dpp v153, v153, v153 row_ror:8 row_mask:0xf bank_mask:0xf
  v_add_f32_dpp v156, v156, v156 row_ror:8 row_mask:0xf bank_mask:0xf
  v_add_f32_dpp v157, v157, v157 row_ror:8 row_mask:0xf bank_mask:0xf
  v_permlane16_swap_b32 v136, v137
  v_permlane16_swap_b32 v140, v141
  v_permlane16_swap_b32 v144, v145
  v_permlane16_swap_b32 v148, v149
  v_permlane16_swap_b32 v152, v153
  v_permlane16_swap_b32 v156, v157
  v_add_f32 v136, v136, v137
  v_add_f32 v140, v140, v141
  v_add_f32 v144, v144, v145
  v_add_f32 v148, v148, v149
  v_add_f32 v152, v152, v153
  v_add_f32 v156, v156, v157
  v_mov_b32 v139, v136
  v_mov_b32 v143, v140
  v_mov_b32 v147, v144
  v_mov_b32 v151, v148
  v_mov_b32 v155, v152
  v_mov_b32 v159, v156
  v_permlane32_swap_b32 v136, v139
  v_permlane32_swap_b32 v140, v143
  v_permlane32_swap_b32 v144, v147
  v_permlane32_swap_b32 v148, v151
  v_permlane32_swap_b32 v152, v155
  v_permlane32_swap_b32 v156, v159
  v_add_f32 v136, v136, v139
  v_add_f32 v140, v140, v143
  v_add_f32 v144, v144, v147
  v_add_f32 v148, v148, v151
  v_add_f32 v152, v152, v155
  v_add_f32 v156, v156, v159
  v_bfe_u32 v18, v0, 8, 1
  v_lshl_or_b32 v19, v9, 1, v18
  v_lshlrev_b32 v19, 2, v19
  s_lshl_b32 s73, s65, 6
  s_add_u32 s73, s73, 157120
  v_add_u32 v19, s73, v19
  v_lshl_or_b32 v18, v9, 1, v18
  v_lshlrev_b32 v18, 4, v18
  s_lshl_b32 s73, s65, 1
  s_add_u32 s73, s73, 156096
  v_add_u32 v18, s73, v18
  s_mov_b32 exec_lo, 0xff00ff
  s_mov_b32 exec_hi, 0x0
  ds_write_b32 v19, v136
  ds_write_b32 v19, v140 offset:512
  s_mov_b64 exec, -1
  v_add_f32_dpp v146, v146, v146 row_ror:8 row_mask:0xf bank_mask:0xf
  v_mov_b32 v16, v146
  s_nop 1
  v_permlane16_swap_b32 v146, v16
  v_add_f32 v146, v146, v16
  v_mov_b32 v16, v146
  s_nop 1
  v_permlane32_swap_b32 v146, v16
  v_add_f32 v146, v146, v16
  s_nop 1
  v_mov_b32_dpp v14, v146 quad_perm:[0,0,2,2] row_mask:0xf bank_mask:0xf
  v_mov_b32_dpp v15, v146 quad_perm:[1,1,3,3] row_mask:0xf bank_mask:0xf
  v_mov_b32 v16, 0x3727c5ac
  v_mul_f32 v14, 0x3acccccd, v14
  v_mul_f32 v15, 0x3acccccd, v15
  v_fma_f32 v15, -v14, v14, v15
  v_max_f32 v15, 0, v15
  v_add_f32 v15, v15, v16
  v_rsq_f32 v15, v15
  v_fma_f32 v144, -v14, v136, v144
  v_mul_f32 v144, v144, v15
  v_cvt_pk_bf16_f32 v144, v144, v144
  s_mov_b32 exec_lo, 0xff00ff
  s_mov_b32 exec_hi, 0x0
  ds_write_b16 v18, v144 offset:0
  s_mov_b64 exec, -1
  v_add_f32_dpp v150, v150, v150 row_ror:8 row_mask:0xf bank_mask:0xf
  v_mov_b32 v16, v150
  s_nop 1
  v_permlane16_swap_b32 v150, v16
  v_add_f32 v150, v150, v16
  v_mov_b32 v16, v150
  s_nop 1
  v_permlane32_swap_b32 v150, v16
  v_add_f32 v150, v150, v16
  s_nop 1
  v_mov_b32_dpp v14, v150 quad_perm:[0,0,2,2] row_mask:0xf bank_mask:0xf
  v_mov_b32_dpp v15, v150 quad_perm:[1,1,3,3] row_mask:0xf bank_mask:0xf
  v_mov_b32 v16, 0x3727c5ac
  v_mul_f32 v14, 0x3acccccd, v14
  v_mul_f32 v15, 0x3acccccd, v15
  v_fma_f32 v15, -v14, v14, v15
  v_max_f32 v15, 0, v15
  v_add_f32 v15, v15, v16
  v_rsq_f32 v15, v15
  v_fma_f32 v148, -v14, v136, v148
)ASM"
R"ASM(
  v_mul_f32 v148, v148, v15
  v_cvt_pk_bf16_f32 v148, v148, v148
  s_mov_b32 exec_lo, 0xff00ff
  s_mov_b32 exec_hi, 0x0
  ds_write_b16 v18, v148 offset:256
  s_mov_b64 exec, -1
  v_add_f32_dpp v154, v154, v154 row_ror:8 row_mask:0xf bank_mask:0xf
  v_mov_b32 v16, v154
  s_nop 1
  v_permlane16_swap_b32 v154, v16
  v_add_f32 v154, v154, v16
  v_mov_b32 v16, v154
  s_nop 1
  v_permlane32_swap_b32 v154, v16
  v_add_f32 v154, v154, v16
  s_nop 1
  v_mov_b32_dpp v14, v154 quad_perm:[0,0,2,2] row_mask:0xf bank_mask:0xf
  v_mov_b32_dpp v15, v154 quad_perm:[1,1,3,3] row_mask:0xf bank_mask:0xf
  v_mov_b32 v16, 0x3727c5ac
  v_mul_f32 v14, 0x3acccccd, v14
  v_mul_f32 v15, 0x3acccccd, v15
  v_fma_f32 v15, -v14, v14, v15
  v_max_f32 v15, 0, v15
  v_add_f32 v15, v15, v16
  v_rsq_f32 v15, v15
  v_fma_f32 v152, -v14, v136, v152
  v_mul_f32 v152, v152, v15
  v_cvt_pk_bf16_f32 v152, v152, v152
  s_mov_b32 exec_lo, 0xff00ff
  s_mov_b32 exec_hi, 0x0
  ds_write_b16 v18, v152 offset:512
  s_mov_b64 exec, -1
  v_add_f32_dpp v158, v158, v158 row_ror:8 row_mask:0xf bank_mask:0xf
  v_mov_b32 v16, v158
  s_nop 1
  v_permlane16_swap_b32 v158, v16
  v_add_f32 v158, v158, v16
  v_mov_b32 v16, v158
  s_nop 1
  v_permlane32_swap_b32 v158, v16
  v_add_f32 v158, v158, v16
  s_nop 1
  v_mov_b32_dpp v14, v158 quad_perm:[0,0,2,2] row_mask:0xf bank_mask:0xf
  v_mov_b32_dpp v15, v158 quad_perm:[1,1,3,3] row_mask:0xf bank_mask:0xf
  v_mov_b32 v16, 0x3727c5ac
  v_mul_f32 v14, 0x3acccccd, v14
  v_mul_f32 v15, 0x3acccccd, v15
  v_fma_f32 v15, -v14, v14, v15
  v_max_f32 v15, 0, v15
  v_add_f32 v15, v15, v16
  v_rsq_f32 v15, v15
  v_fma_f32 v156, -v14, v136, v156
  v_mul_f32 v156, v156, v15
  v_cvt_pk_bf16_f32 v156, v156, v156
  s_mov_b32 exec_lo, 0xff00ff
  s_mov_b32 exec_hi, 0x0
  ds_write_b16 v18, v156 offset:768
  s_mov_b64 exec, -1
  s_waitcnt lgkmcnt(0)
  s_barrier
  s_add_u32 s75, s68, 90112
  buffer_load_dwordx4 v[116:119], v0, s[20:23], s75 offen nt
  s_add_u32 s75, s68, 98304
  buffer_load_dwordx4 v[120:123], v0, s[20:23], s75 offen nt
  s_add_u32 s75, s68, 106496
  buffer_load_dwordx4 v[124:127], v0, s[20:23], s75 offen nt
  s_branch .c227
.c126:
  s_lshl_b32 s72, s64, 2
  s_add_u32 s72, s72, 62
  s_sub_u32 s72, s72, 64
  s_add_u32 s62, s60, s72
  s_lshr_b32 s95, s72, 1
  s_and_b32 s95, s95, 2
  s_or_b32 s95, s95, 0
  s_mul_i32 s95, s95, 589824
  s_add_u32 s62, s62, 1
  s_or_b32 s62, s62, 0x80000000
  v_bfe_u32 v9, v0, 4, 6
  v_lshrrev_b32 v20, 3, v9
  v_mul_u32_u24 v20, 1024, v20
  v_and_b32 v9, 7, v9
  v_lshl_add_u32 v20, v9, 4, v20
  s_mul_i32 s76, s66, 8192
  s_add_u32 s76, s76, s95
  s_add_u32 s76, s76, 327680
  s_lshl_b32 s73, s65, 7
  s_add_u32 s76, s76, s73
  v_bfe_u32 v18, v0, 8, 1
  v_lshl_or_b32 v19, v9, 1, v18
  v_lshlrev_b32 v19, 2, v19
  s_lshl_b32 s73, s65, 6
  s_add_u32 s73, s73, 157632
  v_add_u32 v19, s73, v19
  ds_read_b32 v22, v19
  v_lshl_or_b32 v18, v9, 1, v18
  v_lshlrev_b32 v18, 4, v18
  s_lshl_b32 s73, s65, 1
  s_add_u32 s73, s73, 155840
  v_add_u32 v18, s73, v18
  s_sleep 20
  buffer_load_dwordx4 v[10:13], v20, s[48:51], s76 offen sc1
  s_waitcnt vmcnt(0)
  v_cmp_eq_u32 vcc, s62, v13
  s_nop 0
  s_mov_b64 s[78:79], vcc
  s_cmp_eq_u64 s[78:79], exec
  s_nop 1
  s_cbranch_scc1 .ok30
  s_mov_b32 s94, 0
.re31:
  s_cmp_lg_u32 s71, 0
  s_cbranch_scc1 .ok30
  s_add_u32 s94, s94, 1
  s_cmp_gt_u32 s94, 200000
  s_cselect_b32 s71, 1, 0
  buffer_load_dwordx4 v[10:13], v20, s[48:51], s76 offen sc1
  s_waitcnt vmcnt(0)
  v_cmp_eq_u32 vcc, s62, v13
  s_nop 0
  s_mov_b64 s[78:79], vcc
  s_cmp_eq_u64 s[78:79], exec
  s_nop 1
  s_cbranch_scc0 .re31
.ok30:
  s_add_u32 s75, s68, 65536
  buffer_load_dwordx4 v[104:107], v0, s[20:23], s75 offen nt
  s_add_u32 s75, s68, 73728
  buffer_load_dwordx4 v[108:111], v0, s[20:23], s75 offen nt
  s_add_u32 s75, s68, 81920
  buffer_load_dwordx4 v[112:115], v0, s[20:23], s75 offen nt
  v_add_f32_dpp v12, v12, v12 row_ror:8 row_mask:0xf bank_mask:0xf
  v_add_f32_dpp v10, v10, v10 row_ror:8 row_mask:0xf bank_mask:0xf
  v_add_f32_dpp v11, v11, v11 row_ror:8 row_mask:0xf bank_mask:0xf
  v_mov_b32 v16, v12
  s_nop 1
  v_permlane16_swap_b32 v12, v16
  v_permlane16_swap_b32 v10, v11
  v_add_f32 v12, v12, v16
  v_add_f32 v10, v10, v11
  v_mov_b32 v16, v12
  v_mov_b32 v17, v10
  s_nop 0
  v_permlane32_swap_b32 v12, v16
  v_permlane32_swap_b32 v10, v17
  v_add_f32 v12, v12, v16
  v_add_f32 v10, v10, v17
  s_nop 0
  v_mov_b32_dpp v14, v12 quad_perm:[0,0,2,2] row_mask:0xf bank_mask:0xf
  v_mov_b32_dpp v15, v12 quad_perm:[1,1,3,3] row_mask:0xf bank_mask:0xf
  v_mov_b32 v16, 0x3727c5ac
  v_mul_f32 v14, 0x3acccccd, v14
  v_mul_f32 v15, 0x3acccccd, v15
  v_fma_f32 v15, -v14, v14, v15
  v_max_f32 v15, 0, v15
  v_add_f32 v15, v15, v16
  v_rsq_f32 v15, v15
  s_waitcnt lgkmcnt(0)
  v_fma_f32 v10, -v14, v22, v10
  v_mul_f32 v10, v10, v15
  v_cvt_pk_bf16_f32 v10, v10, v10
  s_mov_b32 exec_lo, 0xff00ff
  s_mov_b32 exec_hi, 0x0
  ds_write_b16 v18, v10
  s_mov_b64 exec, -1
  s_add_u32 s75, s68, 90112
  buffer_load_dwordx4 v[116:119], v0, s[20:23], s75 offen nt
  s_add_u32 s75, s68, 98304
  buffer_load_dwordx4 v[120:123], v0, s[20:23], s75 offen nt
  s_add_u32 s75, s68, 106496
  buffer_load_dwordx4 v[124:127], v0, s[20:23], s75 offen nt
  s_waitcnt lgkmcnt(0)
  s_barrier
.c227:
  s_add_u32 s75, s68, 114688
  buffer_load_dwordx4 v[128:131], v0, s[20:23], s75 offen nt
  s_add_u32 s75, s68, 122880
  buffer_load_dwordx4 v[132:135], v0, s[20:23], s75 offen nt
  s_cmp_eq_u32 s64, 0
  s_cbranch_scc0 .c132
  s_add_u32 s75, s68, 0
  buffer_load_dwordx4 v[136:139], v1, s[24:27], s75 offen nt
  s_add_u32 s75, s68, 8192
  buffer_load_dwordx4 v[140:143], v1, s[24:27], s75 offen nt
  s_add_u32 s75, s68, 16384
  buffer_load_dwordx4 v[144:147], v1, s[24:27], s75 offen nt
  s_add_u32 s75, s68, 24576
  buffer_load_dwordx4 v[148:151], v1, s[24:27], s75 offen nt
  s_branch .c233
.c132:
  s_mov_b32 s76, 155840
  v_and_b32 v9, 0xff, v1
  v_add_u32 v9, s76, v9
  v_mov_b32 v22, 155776
  v_cndmask_b32 v23, v22, v9, s[84:85]
  ds_read_b128 v[24:27], v23
  s_nop 0
  v_cndmask_b32 v23, v22, v9, s[86:87]
  ds_read_b128 v[28:31], v23
  s_nop 0
)ASM"
R"ASM(
  v_cndmask_b32 v23, v22, v9, s[88:89]
  ds_read_b128 v[32:35], v23
  s_nop 0
  v_cndmask_b32 v23, v22, v9, s[90:91]
  ds_read_b128 v[36:39], v23
  s_waitcnt lgkmcnt(0)
  v_mfma_f32_16x16x32_bf16 v[10:13], v[136:139], v[24:27], 0
  v_mfma_f32_16x16x32_bf16 v[10:13], v[140:143], v[28:31], v[10:13]
  v_mfma_f32_16x16x32_bf16 v[10:13], v[144:147], v[32:35], v[10:13]
  v_mfma_f32_16x16x32_bf16 v[10:13], v[148:151], v[36:39], v[10:13]
  v_mfma_f32_16x16x32_bf16 v[14:17], v[168:171], v[24:27], 0
  v_mfma_f32_16x16x32_bf16 v[14:17], v[172:175], v[28:31], v[14:17]
  v_mfma_f32_16x16x32_bf16 v[14:17], v[176:179], v[32:35], v[14:17]
  v_mfma_f32_16x16x32_bf16 v[14:17], v[180:183], v[36:39], v[14:17]
  s_nop 3
  v_cndmask_b32 v22, v10, v11, s[80:81]
  v_cndmask_b32 v23, v12, v13, s[80:81]
  v_mfma_f32_16x16x32_bf16 v[10:13], v[152:155], v[24:27], 0
  v_mfma_f32_16x16x32_bf16 v[10:13], v[156:159], v[28:31], v[10:13]
  v_mfma_f32_16x16x32_bf16 v[10:13], v[160:163], v[32:35], v[10:13]
  v_mfma_f32_16x16x32_bf16 v[10:13], v[164:167], v[36:39], v[10:13]
  v_cndmask_b32 v14, v14, v15, s[80:81]
  v_cndmask_b32 v16, v16, v17, s[80:81]
  v_cndmask_b32 v15, v22, v16, s[82:83]
  v_cndmask_b32 v17, v14, v23, s[82:83]
  s_nop 1
  v_add_f32_dpp v23, v17, v15 quad_perm:[2,3,0,1] row_mask:0xf bank_mask:0xf
  v_mfma_f32_16x16x32_bf16 v[14:17], v[184:187], v[24:27], 0
  v_mfma_f32_16x16x32_bf16 v[14:17], v[188:191], v[28:31], v[14:17]
  v_mfma_f32_16x16x32_bf16 v[14:17], v[192:195], v[32:35], v[14:17]
  v_mfma_f32_16x16x32_bf16 v[14:17], v[196:199], v[36:39], v[14:17]
  v_cndmask_b32 v22, v10, v11, s[80:81]
  v_cndmask_b32 v9, v12, v13, s[80:81]
  s_nop 5
  v_cndmask_b32 v14, v14, v15, s[80:81]
  v_cndmask_b32 v16, v16, v17, s[80:81]
  v_cndmask_b32 v15, v22, v16, s[82:83]
  v_cndmask_b32 v17, v14, v9, s[82:83]
  s_nop 1
  v_add_f32_dpp v22, v17, v15 quad_perm:[2,3,0,1] row_mask:0xf bank_mask:0xf
  v_cndmask_b32 v10, v23, v22, s[80:81]
  v_cndmask_b32 v11, v22, v23, s[80:81]
  s_nop 1
  v_add_f32_dpp v12, v11, v10 quad_perm:[1,0,3,2] row_mask:0xf bank_mask:0xf
  s_nop 1
  v_max_f32_dpp v13, v12, v12 row_ror:8 row_mask:0xf bank_mask:0xf
  s_nop 1
  v_max_f32_dpp v13, v13, v13 row_ror:4 row_mask:0xf bank_mask:0xf
  s_nop 1
  v_max_f32_dpp v13, v13, v13 quad_perm:[2,3,0,1] row_mask:0xf bank_mask:0xf
  s_nop 1
  v_max_f32_dpp v13, v13, v13 quad_perm:[1,0,3,2] row_mask:0xf bank_mask:0xf
  v_mov_b32 v14, v13
  s_nop 1
  v_permlane16_swap_b32 v13, v14
  v_max_f32 v13, v13, v14
  v_mov_b32 v14, v13
  s_nop 1
  v_permlane32_swap_b32 v13, v14
  v_max_f32 v13, v13, v14
  v_mul_f32 v37, s92, v13
  v_fma_f32 v14, v12, s92, -v37
  v_exp_f32 v38, v14
  s_nop 0
  v_cvt_pk_bf16_f32 v36, v38, v38
  s_nop 1
  v_and_b32_dpp v10, v36, v2 row_newbcast:0 row_mask:0xf bank_mask:0xf
  v_and_b32_dpp v11, v36, v3 row_newbcast:0 row_mask:0xf bank_mask:0xf
  v_and_b32_dpp v12, v36, v4 row_newbcast:0 row_mask:0xf bank_mask:0xf
  v_and_b32_dpp v13, v36, v5 row_newbcast:0 row_mask:0xf bank_mask:0xf
  v_and_b32_dpp v14, v36, v2 row_newbcast:4 row_mask:0xf bank_mask:0xf
  v_and_b32_dpp v15, v36, v3 row_newbcast:4 row_mask:0xf bank_mask:0xf
  v_and_b32_dpp v16, v36, v4 row_newbcast:4 row_mask:0xf bank_mask:0xf
  v_and_b32_dpp v17, v36, v5 row_newbcast:4 row_mask:0xf bank_mask:0xf
  v_mfma_f32_16x16x32_bf16 v[18:21], v[200:203], v[10:13], 0
  s_add_u32 s75, s68, 0
  buffer_load_dwordx4 v[136:139], v1, s[24:27], s75 offen nt
  v_and_b32_dpp v10, v36, v2 row_newbcast:8 row_mask:0xf bank_mask:0xf
  v_and_b32_dpp v11, v36, v3 row_newbcast:8 row_mask:0xf bank_mask:0xf
  v_and_b32_dpp v12, v36, v4 row_newbcast:8 row_mask:0xf bank_mask:0xf
  v_and_b32_dpp v13, v36, v5 row_newbcast:8 row_mask:0xf bank_mask:0xf
  v_mfma_f32_16x16x32_bf16 v[18:21], v[204:207], v[14:17], v[18:21]
  v_and_b32_dpp v14, v36, v2 row_newbcast:12 row_mask:0xf bank_mask:0xf
  v_and_b32_dpp v15, v36, v3 row_newbcast:12 row_mask:0xf bank_mask:0xf
  v_and_b32_dpp v16, v36, v4 row_newbcast:12 row_mask:0xf bank_mask:0xf
  v_and_b32_dpp v17, v36, v5 row_newbcast:12 row_mask:0xf bank_mask:0xf
  v_mfma_f32_16x16x32_bf16 v[18:21], v[208:211], v[10:13], v[18:21]
  v_and_b32_dpp v10, v36, v2 row_newbcast:1 row_mask:0xf bank_mask:0xf
  v_and_b32_dpp v11, v36, v3 row_newbcast:1 row_mask:0xf bank_mask:0xf
  v_and_b32_dpp v12, v36, v4 row_newbcast:1 row_mask:0xf bank_mask:0xf
  v_and_b32_dpp v13, v36, v5 row_newbcast:1 row_mask:0xf bank_mask:0xf
  v_mfma_f32_16x16x32_bf16 v[18:21], v[212:215], v[14:17], v[18:21]
  s_add_u32 s75, s68, 8192
  buffer_load_dwordx4 v[140:143], v1, s[24:27], s75 offen nt
  v_and_b32_dpp v14, v36, v2 row_newbcast:5 row_mask:0xf bank_mask:0xf
  v_and_b32_dpp v15, v36, v3 row_newbcast:5 row_mask:0xf bank_mask:0xf
  v_and_b32_dpp v16, v36, v4 row_newbcast:5 row_mask:0xf bank_mask:0xf
  v_and_b32_dpp v17, v36, v5 row_newbcast:5 row_mask:0xf bank_mask:0xf
  v_mfma_f32_16x16x32_bf16 v[18:21], v[216:219], v[10:13], v[18:21]
  v_and_b32_dpp v10, v36, v2 row_newbcast:9 row_mask:0xf bank_mask:0xf
  v_and_b32_dpp v11, v36, v3 row_newbcast:9 row_mask:0xf bank_mask:0xf
  v_and_b32_dpp v12, v36, v4 row_newbcast:9 row_mask:0xf bank_mask:0xf
  v_and_b32_dpp v13, v36, v5 row_newbcast:9 row_mask:0xf bank_mask:0xf
  v_mfma_f32_16x16x32_bf16 v[18:21], v[220:223], v[14:17], v[18:21]
  v_and_b32_dpp v14, v36, v2 row_newbcast:13 row_mask:0xf bank_mask:0xf
  v_and_b32_dpp v15, v36, v3 row_newbcast:13 row_mask:0xf bank_mask:0xf
  v_and_b32_dpp v16, v36, v4 row_newbcast:13 row_mask:0xf bank_mask:0xf
  v_and_b32_dpp v17, v36, v5 row_newbcast:13 row_mask:0xf bank_mask:0xf
  v_mfma_f32_16x16x32_bf16 v[18:21], v[224:227], v[10:13], v[18:21]
  s_add_u32 s75, s68, 16384
  buffer_load_dwordx4 v[144:147], v1, s[24:27], s75 offen nt
  v_and_b32_dpp v10, v36, v2 row_newbcast:2 row_mask:0xf bank_mask:0xf
  v_and_b32_dpp v11, v36, v3 row_newbcast:2 row_mask:0xf bank_mask:0xf
  v_and_b32_dpp v12, v36, v4 row_newbcast:2 row_mask:0xf bank_mask:0xf
  v_and_b32_dpp v13, v36, v5 row_newbcast:2 row_mask:0xf bank_mask:0xf
  v_mfma_f32_16x16x32_bf16 v[18:21], v[228:231], v[14:17], v[18:21]
  v_and_b32_dpp v14, v36, v2 row_newbcast:6 row_mask:0xf bank_mask:0xf
  v_and_b32_dpp v15, v36, v3 row_newbcast:6 row_mask:0xf bank_mask:0xf
  v_and_b32_dpp v16, v36, v4 row_newbcast:6 row_mask:0xf bank_mask:0xf
  v_and_b32_dpp v17, v36, v5 row_newbcast:6 row_mask:0xf bank_mask:0xf
  v_mfma_f32_16x16x32_bf16 v[18:21], v[232:235], v[10:13], v[18:21]
  v_and_b32_dpp v10, v36, v2 row_newbcast:10 row_mask:0xf bank_mask:0xf
  v_and_b32_dpp v11, v36, v3 row_newbcast:10 row_mask:0xf bank_mask:0xf
  v_and_b32_dpp v12, v36, v4 row_newbcast:10 row_mask:0xf bank_mask:0xf
  v_and_b32_dpp v13, v36, v5 row_newbcast:10 row_mask:0xf bank_mask:0xf
  v_mfma_f32_16x16x32_bf16 v[18:21], v[236:239], v[14:17], v[18:21]
  s_add_u32 s75, s68, 24576
  buffer_load_dwordx4 v[148:151], v1, s[24:27], s75 offen nt
  v_and_b32_dpp v14, v36, v2 row_newbcast:14 row_mask:0xf bank_mask:0xf
  v_and_b32_dpp v15, v36, v3 row_newbcast:14 row_mask:0xf bank_mask:0xf
  v_and_b32_dpp v16, v36, v4 row_newbcast:14 row_mask:0xf bank_mask:0xf
  v_and_b32_dpp v17, v36, v5 row_newbcast:14 row_mask:0xf bank_mask:0xf
  v_mfma_f32_16x16x32_bf16 v[18:21], v[240:243], v[10:13], v[18:21]
  v_and_b32_dpp v10, v36, v2 row_newbcast:3 row_mask:0xf bank_mask:0xf
  v_and_b32_dpp v11, v36, v3 row_newbcast:3 row_mask:0xf bank_mask:0xf
  v_and_b32_dpp v12, v36, v4 row_newbcast:3 row_mask:0xf bank_mask:0xf
  v_and_b32_dpp v13, v36, v5 row_newbcast:3 row_mask:0xf bank_mask:0xf
  v_mfma_f32_16x16x32_bf16 v[18:21], v[244:247], v[14:17], v[18:21]
  v_and_b32_dpp v14, v36, v2 row_newbcast:7 row_mask:0xf bank_mask:0xf
  v_and_b32_dpp v15, v36, v3 row_newbcast:7 row_mask:0xf bank_mask:0xf
  v_and_b32_dpp v16, v36, v4 row_newbcast:7 row_mask:0xf bank_mask:0xf
  v_and_b32_dpp v17, v36, v5 row_newbcast:7 row_mask:0xf bank_mask:0xf
  v_mfma_f32_16x16x32_bf16 v[18:21], v[248:251], v[10:13], v[18:21]
  v_and_b32_dpp v10, v36, v2 row_newbcast:11 row_mask:0xf bank_mask:0xf
  v_and_b32_dpp v11, v36, v3 row_newbcast:11 row_mask:0xf bank_mask:0xf
  v_and_b32_dpp v12, v36, v4 row_newbcast:11 row_mask:0xf bank_mask:0xf
  v_and_b32_dpp v13, v36, v5 row_newbcast:11 row_mask:0xf bank_mask:0xf
  v_mfma_f32_16x16x32_bf16 v[18:21], v[252:255], v[14:17], v[18:21]
  v_add_u32 v9, 131072, v0
  ds_read_b128 v[28:31], v9
  s_waitcnt lgkmcnt(0)
  v_and_b32_dpp v14, v36, v2 row_newbcast:15 row_mask:0xf bank_mask:0xf
  v_and_b32_dpp v15, v36, v3 row_newbcast:15 row_mask:0xf bank_mask:0xf
  v_and_b32_dpp v16, v36, v4 row_newbcast:15 row_mask:0xf bank_mask:0xf
  v_and_b32_dpp v17, v36, v5 row_newbcast:15 row_mask:0xf bank_mask:0xf
  v_mfma_f32_16x16x32_bf16 v[18:21], v[28:31], v[10:13], v[18:21]
  v_add_u32 v9, 139264, v0
  ds_read_b128 v[32:35], v9
  s_waitcnt lgkmcnt(0)
  v_mfma_f32_16x16x32_bf16 v[18:21], v[32:35], v[14:17], v[18:21]
  v_add_f32_dpp v22, v38, v38 row_ror:8 row_mask:0xf bank_mask:0xf
  s_nop 1
  v_add_f32_dpp v22, v22, v22 row_ror:4 row_mask:0xf bank_mask:0xf
  s_nop 1
  v_add_f32_dpp v22, v22, v22 quad_perm:[2,3,0,1] row_mask:0xf bank_mask:0xf
  s_nop 1
  v_add_f32_dpp v22, v22, v22 quad_perm:[1,0,3,2] row_mask:0xf bank_mask:0xf
  v_mov_b32 v23, v22
  s_nop 1
  v_permlane16_swap_b32 v22, v23
  v_add_f32 v22, v22, v23
  v_mov_b32 v23, v22
  s_nop 1
  v_permlane32_swap_b32 v22, v23
  v_add_f32 v22, v22, v23
  v_and_b32 v9, 0xf0, v0
  v_lshlrev_b32 v9, 2, v9
  v_bfe_u32 v10, v0, 8, 2
  v_lshl_or_b32 v9, v10, 4, v9
  s_lshl_b32 s72, s65, 9
  s_add_u32 s72, s72, s70
  v_add_u32 v9, s72, v9
  s_mov_b32 exec_lo, 0xff00ff
  s_mov_b32 exec_hi, 0xff00ff
  ds_write_b128 v9, v[18:21]
  s_mov_b32 exec_lo, 0x1
  s_mov_b32 exec_hi, 0x0
  s_lshl_b32 s72, s65, 3
  s_add_u32 s72, s72, s70
  v_mov_b32 v9, s72
  v_mov_b32 v23, v22
  v_mov_b32 v22, v37
  ds_write_b64 v9, v[22:23] offset:4096
  s_mov_b64 exec, -1
  v_bfe_u32 v9, v0, 4, 6
  v_lshlrev_b32 v28, 3, v9
  v_add_u32 v28, s70, v28
  v_and_b32 v29, 7, v9
  v_lshlrev_b32 v29, 3, v29
  v_add_u32 v29, s70, v29
)ASM"
R"ASM(
  v_lshlrev_b32 v9, 4, v9
  s_lshl_b32 s72, s64, 2
  s_add_u32 s72, s72, 63
  s_sub_u32 s72, s72, 64
  s_add_u32 s62, s60, s72
  s_lshr_b32 s95, s72, 1
  s_and_b32 s95, s95, 2
  s_or_b32 s95, s95, 1
  s_mul_i32 s95, s95, 589824
  s_add_u32 s62, s62, 1
  s_or_b32 s62, s62, 0x80000000
  s_add_u32 s72, s95, s96
  s_waitcnt lgkmcnt(0)
  s_barrier
  s_cmp_lg_u32 s65, 0
  s_cbranch_scc1 .mg35
  s_setprio 3
  ds_read_b64 v[26:27], v29 offset:4096
  ds_read_b64 v[10:11], v28 offset:0
  ds_read_b64 v[12:13], v28 offset:512
  ds_read_b64 v[14:15], v28 offset:1024
  ds_read_b64 v[16:17], v28 offset:1536
  ds_read_b64 v[18:19], v28 offset:2048
  ds_read_b64 v[20:21], v28 offset:2560
  ds_read_b64 v[22:23], v28 offset:3072
  ds_read_b64 v[24:25], v28 offset:3584
  s_waitcnt lgkmcnt(8)
  v_mov_b32 v30, v26
  s_nop 1
  v_max_f32_dpp v30, v30, v30 quad_perm:[1,0,3,2] row_mask:0xf bank_mask:0xf
  s_nop 1
  v_max_f32_dpp v30, v30, v30 quad_perm:[2,3,0,1] row_mask:0xf bank_mask:0xf
  s_nop 1
  v_max_f32_dpp v30, v30, v30 row_half_mirror row_mask:0xf bank_mask:0xf
  v_sub_f32 v31, v26, v30
  v_exp_f32 v31, v31
  s_nop 0
  v_mul_f32 v32, v31, v27
  v_mov_b32 v36, 0
  v_mov_b32 v37, 0
  v_mov_b32 v39, s62
  v_add_f32_dpp v32, v32, v32 quad_perm:[1,0,3,2] row_mask:0xf bank_mask:0xf
  s_nop 1
  v_add_f32_dpp v32, v32, v32 quad_perm:[2,3,0,1] row_mask:0xf bank_mask:0xf
  s_nop 1
  v_add_f32_dpp v32, v32, v32 row_half_mirror row_mask:0xf bank_mask:0xf
  s_waitcnt lgkmcnt(0)
  v_fmac_f32_dpp v36, v31, v10 row_newbcast:0 row_mask:0xf bank_mask:0xf
  v_fmac_f32_dpp v37, v31, v11 row_newbcast:0 row_mask:0xf bank_mask:0xf
  s_nop 0
  v_fmac_f32_dpp v36, v31, v12 row_newbcast:1 row_mask:0xf bank_mask:0xf
  v_fmac_f32_dpp v37, v31, v13 row_newbcast:1 row_mask:0xf bank_mask:0xf
  s_nop 0
  v_fmac_f32_dpp v36, v31, v14 row_newbcast:2 row_mask:0xf bank_mask:0xf
  v_fmac_f32_dpp v37, v31, v15 row_newbcast:2 row_mask:0xf bank_mask:0xf
  s_nop 0
  v_fmac_f32_dpp v36, v31, v16 row_newbcast:3 row_mask:0xf bank_mask:0xf
  v_fmac_f32_dpp v37, v31, v17 row_newbcast:3 row_mask:0xf bank_mask:0xf
  s_nop 0
  v_fmac_f32_dpp v36, v31, v18 row_newbcast:4 row_mask:0xf bank_mask:0xf
  v_fmac_f32_dpp v37, v31, v19 row_newbcast:4 row_mask:0xf bank_mask:0xf
  s_nop 0
  v_fmac_f32_dpp v36, v31, v20 row_newbcast:5 row_mask:0xf bank_mask:0xf
  v_fmac_f32_dpp v37, v31, v21 row_newbcast:5 row_mask:0xf bank_mask:0xf
  s_nop 0
  v_fmac_f32_dpp v36, v31, v22 row_newbcast:6 row_mask:0xf bank_mask:0xf
  v_fmac_f32_dpp v37, v31, v23 row_newbcast:6 row_mask:0xf bank_mask:0xf
  s_nop 0
  v_fmac_f32_dpp v36, v31, v24 row_newbcast:7 row_mask:0xf bank_mask:0xf
  v_fmac_f32_dpp v37, v31, v25 row_newbcast:7 row_mask:0xf bank_mask:0xf
  v_cndmask_b32 v38, v30, v32, s[80:81]
  buffer_store_dwordx4 v[36:39], v9, s[48:51], s72 offen sc1
  s_setprio 0
  s_nop 0
.mg35:
  s_xor_b32 s70, s70, 4160
.c233:
  s_add_u32 s75, s68, 32768
  buffer_load_dwordx4 v[152:155], v1, s[24:27], s75 offen nt
  s_add_u32 s75, s68, 40960
  buffer_load_dwordx4 v[156:159], v1, s[24:27], s75 offen nt
  s_add_u32 s75, s68, 49152
  buffer_load_dwordx4 v[160:163], v1, s[24:27], s75 offen nt
  s_add_u32 s75, s68, 57344
  buffer_load_dwordx4 v[164:167], v1, s[24:27], s75 offen nt
  s_waitcnt vmcnt(24)
  s_lshl_b32 s76, s64, 8
  s_add_u32 s76, s76, 156096
  v_and_b32 v9, 0xff, v1
  v_add_u32 v9, s76, v9
  v_mov_b32 v22, 155776
  v_cndmask_b32 v23, v22, v9, s[84:85]
  ds_read_b128 v[24:27], v23
  s_nop 0
  v_cndmask_b32 v23, v22, v9, s[86:87]
  ds_read_b128 v[28:31], v23
  s_nop 0
  v_cndmask_b32 v23, v22, v9, s[88:89]
  ds_read_b128 v[32:35], v23
  s_nop 0
  v_cndmask_b32 v23, v22, v9, s[90:91]
  ds_read_b128 v[36:39], v23
  v_add_u32 v9, s69, v0
  ds_read_b128 v[14:17], v9 offset:0
  ds_read_b128 v[18:21], v9 offset:8192
  s_waitcnt lgkmcnt(2)
  v_mfma_f32_16x16x32_bf16 v[10:13], v[40:43], v[24:27], 0
  v_mfma_f32_16x16x32_bf16 v[10:13], v[44:47], v[28:31], v[10:13]
  v_mfma_f32_16x16x32_bf16 v[10:13], v[48:51], v[32:35], v[10:13]
  v_mfma_f32_16x16x32_bf16 v[10:13], v[52:55], v[36:39], v[10:13]
  s_nop 7
  v_cndmask_b32 v22, v10, v11, s[80:81]
  v_cndmask_b32 v23, v12, v13, s[80:81]
  s_waitcnt lgkmcnt(1)
  v_mfma_f32_16x16x32_bf16 v[10:13], v[14:17], v[24:27], 0
  ds_read_b128 v[14:17], v9 offset:16384
  s_waitcnt lgkmcnt(1)
  v_mfma_f32_16x16x32_bf16 v[10:13], v[18:21], v[28:31], v[10:13]
  ds_read_b128 v[18:21], v9 offset:24576
  s_waitcnt lgkmcnt(1)
  v_mfma_f32_16x16x32_bf16 v[10:13], v[14:17], v[32:35], v[10:13]
  ds_read_b128 v[14:17], v9 offset:32768
  s_waitcnt lgkmcnt(1)
  v_mfma_f32_16x16x32_bf16 v[10:13], v[18:21], v[36:39], v[10:13]
  ds_read_b128 v[18:21], v9 offset:40960
  s_nop 6
  v_cndmask_b32 v10, v10, v11, s[80:81]
  v_cndmask_b32 v12, v12, v13, s[80:81]
  v_cndmask_b32 v11, v10, v23, s[82:83]
  v_cndmask_b32 v13, v22, v12, s[82:83]
  s_nop 1
  v_add_f32_dpp v23, v13, v11 quad_perm:[2,3,0,1] row_mask:0xf bank_mask:0xf
  s_waitcnt lgkmcnt(1)
  v_mfma_f32_16x16x32_bf16 v[10:13], v[14:17], v[24:27], 0
  ds_read_b128 v[14:17], v9 offset:49152
  s_waitcnt lgkmcnt(1)
  v_mfma_f32_16x16x32_bf16 v[10:13], v[18:21], v[28:31], v[10:13]
  ds_read_b128 v[18:21], v9 offset:57344
  s_waitcnt lgkmcnt(1)
  v_mfma_f32_16x16x32_bf16 v[10:13], v[14:17], v[32:35], v[10:13]
  s_waitcnt lgkmcnt(0)
  v_mfma_f32_16x16x32_bf16 v[10:13], v[18:21], v[36:39], v[10:13]
  s_nop 7
  v_cndmask_b32 v22, v10, v11, s[80:81]
  v_cndmask_b32 v9, v12, v13, s[80:81]
  v_mfma_f32_16x16x32_bf16 v[10:13], v[56:59], v[24:27], 0
  v_mfma_f32_16x16x32_bf16 v[10:13], v[60:63], v[28:31], v[10:13]
  v_mfma_f32_16x16x32_bf16 v[10:13], v[64:67], v[32:35], v[10:13]
  v_mfma_f32_16x16x32_bf16 v[10:13], v[68:71], v[36:39], v[10:13]
  s_nop 7
  v_cndmask_b32 v10, v10, v11, s[80:81]
  v_cndmask_b32 v12, v12, v13, s[80:81]
  v_cndmask_b32 v11, v22, v12, s[82:83]
  v_cndmask_b32 v13, v10, v9, s[82:83]
  s_nop 1
  v_add_f32_dpp v22, v13, v11 quad_perm:[2,3,0,1] row_mask:0xf bank_mask:0xf
  v_cndmask_b32 v10, v23, v22, s[80:81]
  v_cndmask_b32 v11, v22, v23, s[80:81]
  s_nop 1
  v_add_f32_dpp v12, v11, v10 quad_perm:[1,0,3,2] row_mask:0xf bank_mask:0xf
  s_nop 1
  v_max_f32_dpp v13, v12, v12 row_ror:8 row_mask:0xf bank_mask:0xf
  s_nop 1
  v_max_f32_dpp v13, v13, v13 row_ror:4 row_mask:0xf bank_mask:0xf
  s_nop 1
  v_max_f32_dpp v13, v13, v13 quad_perm:[2,3,0,1] row_mask:0xf bank_mask:0xf
  s_nop 1
  v_max_f32_dpp v13, v13, v13 quad_perm:[1,0,3,2] row_mask:0xf bank_mask:0xf
  v_mov_b32 v14, v13
  s_nop 1
  v_permlane16_swap_b32 v13, v14
  v_max_f32 v13, v13, v14
  v_mov_b32 v14, v13
  s_nop 1
  v_permlane32_swap_b32 v13, v14
  v_max_f32 v13, v13, v14
  v_mul_f32 v7, s92, v13
  v_fma_f32 v14, v12, s92, -v7
  v_exp_f32 v8, v14
  s_nop 0
  v_cvt_pk_bf16_f32 v6, v8, v8
  s_nop 1
  v_and_b32_dpp v10, v6, v2 row_newbcast:0 row_mask:0xf bank_mask:0xf
  v_and_b32_dpp v11, v6, v3 row_newbcast:0 row_mask:0xf bank_mask:0xf
  v_and_b32_dpp v12, v6, v4 row_newbcast:0 row_mask:0xf bank_mask:0xf
  v_and_b32_dpp v13, v6, v5 row_newbcast:0 row_mask:0xf bank_mask:0xf
  s_waitcnt vmcnt(23)
  v_and_b32_dpp v14, v6, v2 row_newbcast:4 row_mask:0xf bank_mask:0xf
  v_and_b32_dpp v15, v6, v3 row_newbcast:4 row_mask:0xf bank_mask:0xf
  v_and_b32_dpp v16, v6, v4 row_newbcast:4 row_mask:0xf bank_mask:0xf
  v_and_b32_dpp v17, v6, v5 row_newbcast:4 row_mask:0xf bank_mask:0xf
  v_mfma_f32_16x16x32_bf16 v[18:21], v[72:75], v[10:13], 0
  s_waitcnt vmcnt(22)
  v_and_b32_dpp v10, v6, v2 row_newbcast:8 row_mask:0xf bank_mask:0xf
  v_and_b32_dpp v11, v6, v3 row_newbcast:8 row_mask:0xf bank_mask:0xf
  v_and_b32_dpp v12, v6, v4 row_newbcast:8 row_mask:0xf bank_mask:0xf
  v_and_b32_dpp v13, v6, v5 row_newbcast:8 row_mask:0xf bank_mask:0xf
  v_mfma_f32_16x16x32_bf16 v[18:21], v[76:79], v[14:17], v[18:21]
  s_waitcnt vmcnt(21)
  v_and_b32_dpp v14, v6, v2 row_newbcast:12 row_mask:0xf bank_mask:0xf
)ASM"
R"ASM(
  v_and_b32_dpp v15, v6, v3 row_newbcast:12 row_mask:0xf bank_mask:0xf
  v_and_b32_dpp v16, v6, v4 row_newbcast:12 row_mask:0xf bank_mask:0xf
  v_and_b32_dpp v17, v6, v5 row_newbcast:12 row_mask:0xf bank_mask:0xf
  v_mfma_f32_16x16x32_bf16 v[18:21], v[80:83], v[10:13], v[18:21]
  s_waitcnt vmcnt(20)
  s_add_u32 s75, s68, 65536
  buffer_load_dwordx4 v[168:171], v1, s[24:27], s75 offen nt
  v_and_b32_dpp v10, v6, v2 row_newbcast:1 row_mask:0xf bank_mask:0xf
  v_and_b32_dpp v11, v6, v3 row_newbcast:1 row_mask:0xf bank_mask:0xf
  v_and_b32_dpp v12, v6, v4 row_newbcast:1 row_mask:0xf bank_mask:0xf
  v_and_b32_dpp v13, v6, v5 row_newbcast:1 row_mask:0xf bank_mask:0xf
  v_mfma_f32_16x16x32_bf16 v[18:21], v[84:87], v[14:17], v[18:21]
  s_waitcnt vmcnt(20)
  v_and_b32_dpp v14, v6, v2 row_newbcast:5 row_mask:0xf bank_mask:0xf
  v_and_b32_dpp v15, v6, v3 row_newbcast:5 row_mask:0xf bank_mask:0xf
  v_and_b32_dpp v16, v6, v4 row_newbcast:5 row_mask:0xf bank_mask:0xf
  v_and_b32_dpp v17, v6, v5 row_newbcast:5 row_mask:0xf bank_mask:0xf
  v_mfma_f32_16x16x32_bf16 v[18:21], v[88:91], v[10:13], v[18:21]
  s_waitcnt vmcnt(19)
  v_and_b32_dpp v10, v6, v2 row_newbcast:9 row_mask:0xf bank_mask:0xf
  v_and_b32_dpp v11, v6, v3 row_newbcast:9 row_mask:0xf bank_mask:0xf
  v_and_b32_dpp v12, v6, v4 row_newbcast:9 row_mask:0xf bank_mask:0xf
  v_and_b32_dpp v13, v6, v5 row_newbcast:9 row_mask:0xf bank_mask:0xf
  v_mfma_f32_16x16x32_bf16 v[18:21], v[92:95], v[14:17], v[18:21]
  s_waitcnt vmcnt(18)
  v_and_b32_dpp v14, v6, v2 row_newbcast:13 row_mask:0xf bank_mask:0xf
  v_and_b32_dpp v15, v6, v3 row_newbcast:13 row_mask:0xf bank_mask:0xf
  v_and_b32_dpp v16, v6, v4 row_newbcast:13 row_mask:0xf bank_mask:0xf
  v_and_b32_dpp v17, v6, v5 row_newbcast:13 row_mask:0xf bank_mask:0xf
  v_mfma_f32_16x16x32_bf16 v[18:21], v[96:99], v[10:13], v[18:21]
  s_waitcnt vmcnt(17)
  s_add_u32 s75, s68, 73728
  buffer_load_dwordx4 v[172:175], v1, s[24:27], s75 offen nt
  v_and_b32_dpp v10, v6, v2 row_newbcast:2 row_mask:0xf bank_mask:0xf
  v_and_b32_dpp v11, v6, v3 row_newbcast:2 row_mask:0xf bank_mask:0xf
  v_and_b32_dpp v12, v6, v4 row_newbcast:2 row_mask:0xf bank_mask:0xf
  v_and_b32_dpp v13, v6, v5 row_newbcast:2 row_mask:0xf bank_mask:0xf
  v_mfma_f32_16x16x32_bf16 v[18:21], v[100:103], v[14:17], v[18:21]
  s_waitcnt vmcnt(17)
  v_and_b32_dpp v14, v6, v2 row_newbcast:6 row_mask:0xf bank_mask:0xf
  v_and_b32_dpp v15, v6, v3 row_newbcast:6 row_mask:0xf bank_mask:0xf
  v_and_b32_dpp v16, v6, v4 row_newbcast:6 row_mask:0xf bank_mask:0xf
  v_and_b32_dpp v17, v6, v5 row_newbcast:6 row_mask:0xf bank_mask:0xf
  v_mfma_f32_16x16x32_bf16 v[18:21], v[104:107], v[10:13], v[18:21]
  s_waitcnt vmcnt(16)
  v_and_b32_dpp v10, v6, v2 row_newbcast:10 row_mask:0xf bank_mask:0xf
  v_and_b32_dpp v11, v6, v3 row_newbcast:10 row_mask:0xf bank_mask:0xf
  v_and_b32_dpp v12, v6, v4 row_newbcast:10 row_mask:0xf bank_mask:0xf
  v_and_b32_dpp v13, v6, v5 row_newbcast:10 row_mask:0xf bank_mask:0xf
  v_mfma_f32_16x16x32_bf16 v[18:21], v[108:111], v[14:17], v[18:21]
  s_waitcnt vmcnt(15)
  v_and_b32_dpp v14, v6, v2 row_newbcast:14 row_mask:0xf bank_mask:0xf
  v_and_b32_dpp v15, v6, v3 row_newbcast:14 row_mask:0xf bank_mask:0xf
  v_and_b32_dpp v16, v6, v4 row_newbcast:14 row_mask:0xf bank_mask:0xf
  v_and_b32_dpp v17, v6, v5 row_newbcast:14 row_mask:0xf bank_mask:0xf
  v_mfma_f32_16x16x32_bf16 v[18:21], v[112:115], v[10:13], v[18:21]
  s_waitcnt vmcnt(14)
  s_add_u32 s75, s68, 81920
  buffer_load_dwordx4 v[176:179], v1, s[24:27], s75 offen nt
  v_and_b32_dpp v10, v6, v2 row_newbcast:3 row_mask:0xf bank_mask:0xf
  v_and_b32_dpp v11, v6, v3 row_newbcast:3 row_mask:0xf bank_mask:0xf
  v_and_b32_dpp v12, v6, v4 row_newbcast:3 row_mask:0xf bank_mask:0xf
  v_and_b32_dpp v13, v6, v5 row_newbcast:3 row_mask:0xf bank_mask:0xf
  v_mfma_f32_16x16x32_bf16 v[18:21], v[116:119], v[14:17], v[18:21]
  s_waitcnt vmcnt(14)
  v_and_b32_dpp v14, v6, v2 row_newbcast:7 row_mask:0xf bank_mask:0xf
  v_and_b32_dpp v15, v6, v3 row_newbcast:7 row_mask:0xf bank_mask:0xf
  v_and_b32_dpp v16, v6, v4 row_newbcast:7 row_mask:0xf bank_mask:0xf
  v_and_b32_dpp v17, v6, v5 row_newbcast:7 row_mask:0xf bank_mask:0xf
  v_mfma_f32_16x16x32_bf16 v[18:21], v[120:123], v[10:13], v[18:21]
  s_waitcnt vmcnt(13)
  v_and_b32_dpp v10, v6, v2 row_newbcast:11 row_mask:0xf bank_mask:0xf
  v_and_b32_dpp v11, v6, v3 row_newbcast:11 row_mask:0xf bank_mask:0xf
  v_and_b32_dpp v12, v6, v4 row_newbcast:11 row_mask:0xf bank_mask:0xf
  v_and_b32_dpp v13, v6, v5 row_newbcast:11 row_mask:0xf bank_mask:0xf
  v_mfma_f32_16x16x32_bf16 v[18:21], v[124:127], v[14:17], v[18:21]
  s_waitcnt vmcnt(12)
  v_and_b32_dpp v14, v6, v2 row_newbcast:15 row_mask:0xf bank_mask:0xf
  v_and_b32_dpp v15, v6, v3 row_newbcast:15 row_mask:0xf bank_mask:0xf
  v_and_b32_dpp v16, v6, v4 row_newbcast:15 row_mask:0xf bank_mask:0xf
  v_and_b32_dpp v17, v6, v5 row_newbcast:15 row_mask:0xf bank_mask:0xf
  v_mfma_f32_16x16x32_bf16 v[18:21], v[128:131], v[10:13], v[18:21]
  s_waitcnt vmcnt(11)
  s_add_u32 s75, s68, 90112
  buffer_load_dwordx4 v[180:183], v1, s[24:27], s75 offen nt
  v_mfma_f32_16x16x32_bf16 v[18:21], v[132:135], v[14:17], v[18:21]
  v_add_f32_dpp v22, v8, v8 row_ror:8 row_mask:0xf bank_mask:0xf
  s_nop 1
  v_add_f32_dpp v22, v22, v22 row_ror:4 row_mask:0xf bank_mask:0xf
  s_nop 1
  v_add_f32_dpp v22, v22, v22 quad_perm:[2,3,0,1] row_mask:0xf bank_mask:0xf
  s_nop 1
  v_add_f32_dpp v22, v22, v22 quad_perm:[1,0,3,2] row_mask:0xf bank_mask:0xf
  v_mov_b32 v23, v22
  s_nop 1
  v_permlane16_swap_b32 v22, v23
  v_add_f32 v22, v22, v23
  v_mov_b32 v23, v22
  s_nop 1
  v_permlane32_swap_b32 v22, v23
  v_add_f32 v22, v22, v23
  v_and_b32 v9, 0xf0, v0
  v_lshlrev_b32 v9, 2, v9
  v_bfe_u32 v10, v0, 8, 2
  v_lshl_or_b32 v9, v10, 4, v9
  s_lshl_b32 s72, s65, 9
  s_add_u32 s72, s72, s70
  v_add_u32 v9, s72, v9
  s_mov_b32 exec_lo, 0xff00ff
  s_mov_b32 exec_hi, 0xff00ff
  ds_write_b128 v9, v[18:21]
  s_mov_b32 exec_lo, 0x1
  s_mov_b32 exec_hi, 0x0
  s_lshl_b32 s72, s65, 3
  s_add_u32 s72, s72, s70
  v_mov_b32 v9, s72
  v_mov_b32 v23, v22
  v_mov_b32 v22, v7
  ds_write_b64 v9, v[22:23] offset:4096
  s_mov_b64 exec, -1
  v_bfe_u32 v9, v0, 4, 6
  v_lshlrev_b32 v28, 3, v9
  v_add_u32 v28, s70, v28
  v_and_b32 v29, 7, v9
  v_lshlrev_b32 v29, 3, v29
  v_add_u32 v29, s70, v29
  v_lshlrev_b32 v9, 4, v9
  s_lshl_b32 s72, s64, 2
  s_add_u32 s72, s72, 64
  s_sub_u32 s72, s72, 64
  s_add_u32 s62, s60, s72
  s_lshr_b32 s95, s72, 1
  s_and_b32 s95, s95, 2
  s_or_b32 s95, s95, 0
  s_mul_i32 s95, s95, 589824
  s_add_u32 s62, s62, 1
  s_or_b32 s62, s62, 0x80000000
  s_add_u32 s72, s95, s96
  s_waitcnt lgkmcnt(0)
  s_barrier
  s_cmp_lg_u32 s65, 0
  s_cbranch_scc1 .mg36
  s_setprio 3
  ds_read_b64 v[26:27], v29 offset:4096
  ds_read_b64 v[10:11], v28 offset:0
  ds_read_b64 v[12:13], v28 offset:512
  ds_read_b64 v[14:15], v28 offset:1024
  ds_read_b64 v[16:17], v28 offset:1536
  ds_read_b64 v[18:19], v28 offset:2048
  ds_read_b64 v[20:21], v28 offset:2560
  ds_read_b64 v[22:23], v28 offset:3072
  ds_read_b64 v[24:25], v28 offset:3584
  s_waitcnt lgkmcnt(8)
  v_mov_b32 v30, v26
  s_nop 1
  v_max_f32_dpp v30, v30, v30 quad_perm:[1,0,3,2] row_mask:0xf bank_mask:0xf
  s_nop 1
  v_max_f32_dpp v30, v30, v30 quad_perm:[2,3,0,1] row_mask:0xf bank_mask:0xf
  s_nop 1
  v_max_f32_dpp v30, v30, v30 row_half_mirror row_mask:0xf bank_mask:0xf
  v_sub_f32 v31, v26, v30
  v_exp_f32 v31, v31
  s_nop 0
  v_mul_f32 v32, v31, v27
  v_mov_b32 v36, 0
  v_mov_b32 v37, 0
  v_mov_b32 v39, s62
  v_add_f32_dpp v32, v32, v32 quad_perm:[1,0,3,2] row_mask:0xf bank_mask:0xf
  s_nop 1
  v_add_f32_dpp v32, v32, v32 quad_perm:[2,3,0,1] row_mask:0xf bank_mask:0xf
  s_nop 1
  v_add_f32_dpp v32, v32, v32 row_half_mirror row_mask:0xf bank_mask:0xf
  s_waitcnt lgkmcnt(0)
  v_fmac_f32_dpp v36, v31, v10 row_newbcast:0 row_mask:0xf bank_mask:0xf
  v_fmac_f32_dpp v37, v31, v11 row_newbcast:0 row_mask:0xf bank_mask:0xf
  s_nop 0
  v_fmac_f32_dpp v36, v31, v12 row_newbcast:1 row_mask:0xf bank_mask:0xf
  v_fmac_f32_dpp v37, v31, v13 row_newbcast:1 row_mask:0xf bank_mask:0xf
  s_nop 0
  v_fmac_f32_dpp v36, v31, v14 row_newbcast:2 row_mask:0xf bank_mask:0xf
  v_fmac_f32_dpp v37, v31, v15 row_newbcast:2 row_mask:0xf bank_mask:0xf
  s_nop 0
  v_fmac_f32_dpp v36, v31, v16 row_newbcast:3 row_mask:0xf bank_mask:0xf
  v_fmac_f32_dpp v37, v31, v17 row_newbcast:3 row_mask:0xf bank_mask:0xf
  s_nop 0
  v_fmac_f32_dpp v36, v31, v18 row_newbcast:4 row_mask:0xf bank_mask:0xf
  v_fmac_f32_dpp v37, v31, v19 row_newbcast:4 row_mask:0xf bank_mask:0xf
  s_nop 0
  v_fmac_f32_dpp v36, v31, v20 row_newbcast:5 row_mask:0xf bank_mask:0xf
  v_fmac_f32_dpp v37, v31, v21 row_newbcast:5 row_mask:0xf bank_mask:0xf
  s_nop 0
  v_fmac_f32_dpp v36, v31, v22 row_newbcast:6 row_mask:0xf bank_mask:0xf
  v_fmac_f32_dpp v37, v31, v23 row_newbcast:6 row_mask:0xf bank_mask:0xf
  s_nop 0
  v_fmac_f32_dpp v36, v31, v24 row_newbcast:7 row_mask:0xf bank_mask:0xf
  v_fmac_f32_dpp v37, v31, v25 row_newbcast:7 row_mask:0xf bank_mask:0xf
  v_cndmask_b32 v38, v30, v32, s[80:81]
  buffer_store_dwordx4 v[36:39], v9, s[48:51], s72 offen sc1
  s_setprio 0
  s_nop 0
)ASM"
R"ASM(
.mg36:
  s_xor_b32 s70, s70, 4160
  s_lshl_b32 s72, s64, 2
  s_add_u32 s72, s72, 64
  s_sub_u32 s72, s72, 64
  s_add_u32 s62, s60, s72
  s_lshr_b32 s95, s72, 1
  s_and_b32 s95, s95, 2
  s_or_b32 s95, s95, 0
  s_mul_i32 s95, s95, 589824
  s_add_u32 s62, s62, 1
  s_or_b32 s62, s62, 0x80000000
  v_bfe_u32 v19, v0, 8, 1
  v_mul_u32_u24 v19, 10240, v19
  v_bfe_u32 v18, v0, 9, 1
  v_mul_u32_u24 v18, 81920, v18
  v_add_u32 v19, v19, v18
  v_and_b32 v18, 0xf0, v0
  v_add_u32 v19, v19, v18
  s_mul_i32 s73, s65, 1280
  s_mov_b32 exec_lo, 0x3ff03ff
  s_mov_b32 exec_hi, 0x3ff03ff
  s_add_u32 s74, s73, 0
  buffer_load_dwordx4 v[24:27], v19, s[32:35], s74 offen
  s_add_u32 s74, s73, 20480
  buffer_load_dwordx4 v[28:31], v19, s[32:35], s74 offen
  s_add_u32 s74, s73, 40960
  buffer_load_dwordx4 v[32:35], v19, s[32:35], s74 offen
  s_add_u32 s74, s73, 61440
  buffer_load_dwordx4 v[36:39], v19, s[32:35], s74 offen
  s_mov_b64 exec, -1
  v_bfe_u32 v8, v0, 4, 6
  v_mul_u32_u24 v6, 52, v8
  v_lshrrev_b32 v6, 8, v6
  v_mul_u32_u24 v7, 5, v6
  v_sub_u32 v7, v8, v7
  v_lshlrev_b32 v6, 6, v6
  v_lshl_add_u32 v6, v7, 3, v6
  v_add_u32 v6, s70, v6
  v_mul_u32_u24 v7, 12, v8
  v_lshlrev_b32 v8, 4, v8
  v_bfe_u32 v9, v0, 4, 6
  v_lshrrev_b32 v20, 3, v9
  v_mul_u32_u24 v20, 1024, v20
  v_and_b32 v9, 7, v9
  v_lshl_add_u32 v20, v9, 4, v20
  s_mul_i32 s76, s66, 8192
  s_add_u32 s76, s76, s95
  s_lshl_b32 s73, s65, 7
  s_add_u32 s76, s76, s73
  s_sleep 12
  buffer_load_dwordx4 v[10:13], v20, s[48:51], s76 offen sc1
  s_waitcnt vmcnt(0)
  v_cmp_eq_u32 vcc, s62, v13
  s_nop 0
  s_mov_b64 s[78:79], vcc
  s_cmp_eq_u64 s[78:79], exec
  s_nop 1
  s_cbranch_scc1 .ok37
  s_mov_b32 s94, 0
.re38:
  s_cmp_lg_u32 s71, 0
  s_cbranch_scc1 .ok37
  s_add_u32 s94, s94, 1
  s_cmp_gt_u32 s94, 200000
  s_cselect_b32 s71, 1, 0
  buffer_load_dwordx4 v[10:13], v20, s[48:51], s76 offen sc1
  s_waitcnt vmcnt(0)
  v_cmp_eq_u32 vcc, s62, v13
  s_nop 0
  s_mov_b64 s[78:79], vcc
  s_cmp_eq_u64 s[78:79], exec
  s_nop 1
  s_cbranch_scc0 .re38
.ok37:
  s_add_u32 s75, s68, 98304
  buffer_load_dwordx4 v[184:187], v1, s[24:27], s75 offen nt
  s_add_u32 s75, s68, 106496
  buffer_load_dwordx4 v[188:191], v1, s[24:27], s75 offen nt
  s_add_u32 s75, s68, 114688
  buffer_load_dwordx4 v[192:195], v1, s[24:27], s75 offen nt
  v_mov_b32_dpp v14, v12 quad_perm:[0,0,2,2] row_mask:0xf bank_mask:0xf
  v_mov_b32_dpp v15, v12 quad_perm:[1,1,3,3] row_mask:0xf bank_mask:0xf
  v_mov_b32 v20, v14
  s_nop 1
  v_max_f32_dpp v20, v20, v20 row_ror:8 row_mask:0xf bank_mask:0xf
  v_mov_b32 v21, v20
  s_nop 1
  v_permlane16_swap_b32 v20, v21
  v_max_f32 v20, v20, v21
  v_mov_b32 v21, v20
  s_nop 1
  v_permlane32_swap_b32 v20, v21
  v_max_f32 v20, v20, v21
  v_sub_f32 v21, v14, v20
  v_exp_f32 v21, v21
  s_nop 0
  v_mul_f32 v22, v21, v15
  v_mul_f32 v10, v10, v21
  v_mul_f32 v11, v11, v21
  v_add_f32_dpp v22, v22, v22 row_ror:8 row_mask:0xf bank_mask:0xf
  v_add_f32_dpp v10, v10, v10 row_ror:8 row_mask:0xf bank_mask:0xf
  v_add_f32_dpp v11, v11, v11 row_ror:8 row_mask:0xf bank_mask:0xf
  v_mov_b32 v23, v22
  s_nop 1
  v_permlane16_swap_b32 v22, v23
  v_permlane16_swap_b32 v10, v11
  v_add_f32 v22, v22, v23
  v_add_f32 v10, v10, v11
  v_mov_b32 v23, v22
  v_mov_b32 v12, v10
  s_nop 0
  v_permlane32_swap_b32 v22, v23
  v_permlane32_swap_b32 v10, v12
  v_add_f32 v22, v22, v23
  v_add_f32 v10, v10, v12
  v_rcp_f32 v22, v22
  s_nop 0
  v_mov_b32_dpp v10, v10 row_shl:4 row_mask:0xc bank_mask:0xf
  v_mul_f32 v10, v10, v22
  v_cvt_pk_bf16_f32 v18, v10, v10
  s_nop 1
  v_and_b32_dpp v10, v18, v2 row_newbcast:0 row_mask:0xf bank_mask:0xf
  v_and_b32_dpp v11, v18, v3 row_newbcast:0 row_mask:0xf bank_mask:0xf
  v_and_b32_dpp v12, v18, v4 row_newbcast:0 row_mask:0xf bank_mask:0xf
  v_and_b32_dpp v13, v18, v5 row_newbcast:0 row_mask:0xf bank_mask:0xf
  v_and_b32_dpp v14, v18, v2 row_newbcast:1 row_mask:0xf bank_mask:0xf
  v_and_b32_dpp v15, v18, v3 row_newbcast:1 row_mask:0xf bank_mask:0xf
  v_and_b32_dpp v16, v18, v4 row_newbcast:1 row_mask:0xf bank_mask:0xf
  v_and_b32_dpp v17, v18, v5 row_newbcast:1 row_mask:0xf bank_mask:0xf
  v_mfma_f32_16x16x32_bf16 v[20:23], v[24:27], v[10:13], 0
  v_and_b32_dpp v10, v18, v2 row_newbcast:2 row_mask:0xf bank_mask:0xf
  v_and_b32_dpp v11, v18, v3 row_newbcast:2 row_mask:0xf bank_mask:0xf
  v_and_b32_dpp v12, v18, v4 row_newbcast:2 row_mask:0xf bank_mask:0xf
  v_and_b32_dpp v13, v18, v5 row_newbcast:2 row_mask:0xf bank_mask:0xf
  v_mfma_f32_16x16x32_bf16 v[20:23], v[28:31], v[14:17], v[20:23]
  v_and_b32_dpp v14, v18, v2 row_newbcast:3 row_mask:0xf bank_mask:0xf
  v_and_b32_dpp v15, v18, v3 row_newbcast:3 row_mask:0xf bank_mask:0xf
  v_and_b32_dpp v16, v18, v4 row_newbcast:3 row_mask:0xf bank_mask:0xf
  v_and_b32_dpp v17, v18, v5 row_newbcast:3 row_mask:0xf bank_mask:0xf
  v_mfma_f32_16x16x32_bf16 v[20:23], v[32:35], v[10:13], v[20:23]
  v_and_b32 v9, 0xf0, v0
  v_lshlrev_b32 v9, 2, v9
  v_bfe_u32 v19, v0, 8, 2
  v_lshl_or_b32 v9, v19, 4, v9
  s_lshl_b32 s72, s65, 9
  s_add_u32 s72, s72, s70
  v_add_u32 v9, s72, v9
  v_mfma_f32_16x16x32_bf16 v[20:23], v[36:39], v[14:17], v[20:23]
  s_mov_b32 exec_lo, 0xff00ff
  s_mov_b32 exec_hi, 0xff00ff
  s_nop 5
  ds_write_b128 v9, v[20:23]
  s_mov_b64 exec, -1
  s_add_u32 s75, s68, 122880
  buffer_load_dwordx4 v[196:199], v1, s[24:27], s75 offen nt
  s_add_u32 s75, s68, 0
  buffer_load_dwordx4 v[200:203], v0, s[28:31], s75 offen nt
  s_add_u32 s72, s95, s97
  s_waitcnt lgkmcnt(0)
  s_barrier
  s_cmp_lg_u32 s65, 1
  s_cbranch_scc1 .ya39
  s_setprio 3
  s_mov_b32 exec_lo, 0xffffffff
  s_mov_b32 exec_hi, 0xff
  ds_read_b64 v[12:13], v6 offset:0
  ds_read_b64 v[14:15], v6 offset:512
  ds_read_b64 v[16:17], v6 offset:1024
  ds_read_b64 v[18:19], v6 offset:1536
  ds_read_b64 v[20:21], v6 offset:2048
  ds_read_b64 v[22:23], v6 offset:2560
  ds_read_b64 v[24:25], v6 offset:3072
  ds_read_b64 v[26:27], v6 offset:3584
  s_mov_b32 s78, 0xffffffff
  s_mov_b32 s79, 0xff
  s_waitcnt lgkmcnt(4)
  v_add_f32 v12, v12, v14
  v_add_f32 v13, v13, v15
  v_add_f32 v16, v16, v18
  v_add_f32 v17, v17, v19
  s_waitcnt lgkmcnt(0)
  v_add_f32 v20, v20, v22
  v_add_f32 v21, v21, v23
  v_add_f32 v24, v24, v26
  v_add_f32 v25, v25, v27
  v_add_f32 v12, v12, v16
  v_add_f32 v13, v13, v17
  v_add_f32 v20, v20, v24
  v_add_f32 v21, v21, v25
  v_add_f32 v12, v12, v20
  v_add_f32 v13, v13, v21
  v_cvt_pk_bf16_f32 v16, v12, v13
  s_mov_b64 exec, -1
  v_cndmask_b32 v16, 0, v16, s[78:79]
  ds_bpermute_b32 v12, v7, v16
  ds_bpermute_b32 v13, v7, v16 offset:4
  ds_bpermute_b32 v14, v7, v16 offset:8
  v_mov_b32 v15, s62
  s_waitcnt lgkmcnt(0)
)ASM"
R"ASM(
  s_mov_b32 exec_lo, 0xffff
  s_mov_b32 exec_hi, 0x0
  buffer_store_dwordx4 v[12:15], v8, s[48:51], s72 offen sc1
  s_mov_b64 exec, -1
  s_setprio 0
.ya39:
  s_xor_b32 s70, s70, 4160
  s_cmp_eq_u32 s64, 0
  s_cbranch_scc0 .c143
  s_add_u32 s75, s68, 8192
  buffer_load_dwordx4 v[204:207], v0, s[28:31], s75 offen nt
  s_add_u32 s75, s68, 16384
  buffer_load_dwordx4 v[208:211], v0, s[28:31], s75 offen nt
  s_add_u32 s75, s68, 24576
  buffer_load_dwordx4 v[212:215], v0, s[28:31], s75 offen nt
  s_add_u32 s75, s68, 32768
  buffer_load_dwordx4 v[216:219], v0, s[28:31], s75 offen nt
  s_add_u32 s75, s68, 40960
  buffer_load_dwordx4 v[220:223], v0, s[28:31], s75 offen nt
  s_branch .c244
.c143:
  s_lshl_b32 s72, s64, 2
  s_add_u32 s72, s72, 63
  s_sub_u32 s72, s72, 64
  s_add_u32 s62, s60, s72
  s_lshr_b32 s95, s72, 1
  s_and_b32 s95, s95, 2
  s_or_b32 s95, s95, 1
  s_mul_i32 s95, s95, 589824
  s_add_u32 s62, s62, 1
  s_or_b32 s62, s62, 0x80000000
  v_bfe_u32 v19, v0, 8, 1
  v_mul_u32_u24 v19, 10240, v19
  v_bfe_u32 v18, v0, 9, 1
  v_mul_u32_u24 v18, 81920, v18
  v_add_u32 v19, v19, v18
  v_and_b32 v18, 0xf0, v0
  v_add_u32 v19, v19, v18
  s_mul_i32 s73, s65, 1280
  s_mov_b32 exec_lo, 0x3ff03ff
  s_mov_b32 exec_hi, 0x3ff03ff
  s_add_u32 s74, s73, 0
  buffer_load_dwordx4 v[24:27], v19, s[36:39], s74 offen
  s_add_u32 s74, s73, 20480
  buffer_load_dwordx4 v[28:31], v19, s[36:39], s74 offen
  s_add_u32 s74, s73, 40960
  buffer_load_dwordx4 v[32:35], v19, s[36:39], s74 offen
  s_add_u32 s74, s73, 61440
  buffer_load_dwordx4 v[36:39], v19, s[36:39], s74 offen
  s_mov_b64 exec, -1
  v_bfe_u32 v8, v0, 4, 6
  v_mul_u32_u24 v6, 52, v8
  v_lshrrev_b32 v6, 8, v6
  v_mul_u32_u24 v7, 5, v6
  v_sub_u32 v7, v8, v7
  v_lshlrev_b32 v6, 6, v6
  v_lshl_add_u32 v6, v7, 3, v6
  v_add_u32 v6, s70, v6
  v_mul_u32_u24 v7, 12, v8
  v_lshlrev_b32 v8, 4, v8
  v_bfe_u32 v9, v0, 4, 6
  v_lshrrev_b32 v20, 3, v9
  v_mul_u32_u24 v20, 1024, v20
  v_and_b32 v9, 7, v9
  v_lshl_add_u32 v20, v9, 4, v20
  s_mul_i32 s76, s66, 8192
  s_add_u32 s76, s76, s95
  s_lshl_b32 s73, s65, 7
  s_add_u32 s76, s76, s73
  buffer_load_dwordx4 v[10:13], v20, s[48:51], s76 offen sc1
  s_waitcnt vmcnt(0)
  v_cmp_eq_u32 vcc, s62, v13
  s_nop 0
  s_mov_b64 s[78:79], vcc
  s_cmp_eq_u64 s[78:79], exec
  s_nop 1
  s_cbranch_scc1 .ok45
  s_mov_b32 s94, 0
.re46:
  s_cmp_lg_u32 s71, 0
  s_cbranch_scc1 .ok45
  s_add_u32 s94, s94, 1
  s_cmp_gt_u32 s94, 200000
  s_cselect_b32 s71, 1, 0
  buffer_load_dwordx4 v[10:13], v20, s[48:51], s76 offen sc1
  s_waitcnt vmcnt(0)
  v_cmp_eq_u32 vcc, s62, v13
  s_nop 0
  s_mov_b64 s[78:79], vcc
  s_cmp_eq_u64 s[78:79], exec
  s_nop 1
  s_cbranch_scc0 .re46
.ok45:
  s_add_u32 s75, s68, 8192
  buffer_load_dwordx4 v[204:207], v0, s[28:31], s75 offen nt
  s_add_u32 s75, s68, 16384
  buffer_load_dwordx4 v[208:211], v0, s[28:31], s75 offen nt
  s_add_u32 s75, s68, 24576
  buffer_load_dwordx4 v[212:215], v0, s[28:31], s75 offen nt
  v_mov_b32_dpp v14, v12 quad_perm:[0,0,2,2] row_mask:0xf bank_mask:0xf
  v_mov_b32_dpp v15, v12 quad_perm:[1,1,3,3] row_mask:0xf bank_mask:0xf
  v_mov_b32 v20, v14
  s_nop 1
  v_max_f32_dpp v20, v20, v20 row_ror:8 row_mask:0xf bank_mask:0xf
  v_mov_b32 v21, v20
  s_nop 1
  v_permlane16_swap_b32 v20, v21
  v_max_f32 v20, v20, v21
  v_mov_b32 v21, v20
  s_nop 1
  v_permlane32_swap_b32 v20, v21
  v_max_f32 v20, v20, v21
  v_sub_f32 v21, v14, v20
  v_exp_f32 v21, v21
  s_nop 0
  v_mul_f32 v22, v21, v15
  v_mul_f32 v10, v10, v21
  v_mul_f32 v11, v11, v21
  v_add_f32_dpp v22, v22, v22 row_ror:8 row_mask:0xf bank_mask:0xf
  v_add_f32_dpp v10, v10, v10 row_ror:8 row_mask:0xf bank_mask:0xf
  v_add_f32_dpp v11, v11, v11 row_ror:8 row_mask:0xf bank_mask:0xf
  v_mov_b32 v23, v22
  s_nop 1
  v_permlane16_swap_b32 v22, v23
  v_permlane16_swap_b32 v10, v11
  v_add_f32 v22, v22, v23
  v_add_f32 v10, v10, v11
  v_mov_b32 v23, v22
  v_mov_b32 v12, v10
  s_nop 0
  v_permlane32_swap_b32 v22, v23
  v_permlane32_swap_b32 v10, v12
  v_add_f32 v22, v22, v23
  v_add_f32 v10, v10, v12
  v_rcp_f32 v22, v22
  s_nop 0
  v_mov_b32_dpp v10, v10 row_shl:4 row_mask:0xc bank_mask:0xf
  v_mul_f32 v10, v10, v22
  v_cvt_pk_bf16_f32 v18, v10, v10
  s_nop 1
  v_and_b32_dpp v10, v18, v2 row_newbcast:0 row_mask:0xf bank_mask:0xf
  v_and_b32_dpp v11, v18, v3 row_newbcast:0 row_mask:0xf bank_mask:0xf
  v_and_b32_dpp v12, v18, v4 row_newbcast:0 row_mask:0xf bank_mask:0xf
  v_and_b32_dpp v13, v18, v5 row_newbcast:0 row_mask:0xf bank_mask:0xf
  v_and_b32_dpp v14, v18, v2 row_newbcast:1 row_mask:0xf bank_mask:0xf
  v_and_b32_dpp v15, v18, v3 row_newbcast:1 row_mask:0xf bank_mask:0xf
  v_and_b32_dpp v16, v18, v4 row_newbcast:1 row_mask:0xf bank_mask:0xf
  v_and_b32_dpp v17, v18, v5 row_newbcast:1 row_mask:0xf bank_mask:0xf
  v_mfma_f32_16x16x32_bf16 v[20:23], v[24:27], v[10:13], 0
  v_and_b32_dpp v10, v18, v2 row_newbcast:2 row_mask:0xf bank_mask:0xf
  v_and_b32_dpp v11, v18, v3 row_newbcast:2 row_mask:0xf bank_mask:0xf
  v_and_b32_dpp v12, v18, v4 row_newbcast:2 row_mask:0xf bank_mask:0xf
  v_and_b32_dpp v13, v18, v5 row_newbcast:2 row_mask:0xf bank_mask:0xf
  v_mfma_f32_16x16x32_bf16 v[20:23], v[28:31], v[14:17], v[20:23]
  v_and_b32_dpp v14, v18, v2 row_newbcast:3 row_mask:0xf bank_mask:0xf
  v_and_b32_dpp v15, v18, v3 row_newbcast:3 row_mask:0xf bank_mask:0xf
  v_and_b32_dpp v16, v18, v4 row_newbcast:3 row_mask:0xf bank_mask:0xf
  v_and_b32_dpp v17, v18, v5 row_newbcast:3 row_mask:0xf bank_mask:0xf
  v_mfma_f32_16x16x32_bf16 v[20:23], v[32:35], v[10:13], v[20:23]
  v_and_b32 v9, 0xf0, v0
  v_lshlrev_b32 v9, 2, v9
  v_bfe_u32 v19, v0, 8, 2
  v_lshl_or_b32 v9, v19, 4, v9
  s_lshl_b32 s72, s65, 9
  s_add_u32 s72, s72, s70
  v_add_u32 v9, s72, v9
  v_mfma_f32_16x16x32_bf16 v[20:23], v[36:39], v[14:17], v[20:23]
  s_mov_b32 exec_lo, 0xff00ff
  s_mov_b32 exec_hi, 0xff00ff
  s_nop 5
  ds_write_b128 v9, v[20:23]
  s_mov_b64 exec, -1
  s_add_u32 s75, s68, 32768
  buffer_load_dwordx4 v[216:219], v0, s[28:31], s75 offen nt
  s_add_u32 s75, s68, 40960
  buffer_load_dwordx4 v[220:223], v0, s[28:31], s75 offen nt
  s_add_u32 s72, s95, s97
  s_waitcnt lgkmcnt(0)
  s_barrier
  s_cmp_lg_u32 s65, 1
  s_cbranch_scc1 .ya47
  s_setprio 3
  s_mov_b32 exec_lo, 0xffffffff
  s_mov_b32 exec_hi, 0xff
  ds_read_b64 v[12:13], v6 offset:0
  ds_read_b64 v[14:15], v6 offset:512
  ds_read_b64 v[16:17], v6 offset:1024
  ds_read_b64 v[18:19], v6 offset:1536
  ds_read_b64 v[20:21], v6 offset:2048
  ds_read_b64 v[22:23], v6 offset:2560
  ds_read_b64 v[24:25], v6 offset:3072
  ds_read_b64 v[26:27], v6 offset:3584
  s_mov_b32 s78, 0xffffffff
  s_mov_b32 s79, 0xff
  s_waitcnt lgkmcnt(4)
  v_add_f32 v12, v12, v14
  v_add_f32 v13, v13, v15
  v_add_f32 v16, v16, v18
  v_add_f32 v17, v17, v19
  s_waitcnt lgkmcnt(0)
)ASM"
R"ASM(
  v_add_f32 v20, v20, v22
  v_add_f32 v21, v21, v23
  v_add_f32 v24, v24, v26
  v_add_f32 v25, v25, v27
  v_add_f32 v12, v12, v16
  v_add_f32 v13, v13, v17
  v_add_f32 v20, v20, v24
  v_add_f32 v21, v21, v25
  v_add_f32 v12, v12, v20
  v_add_f32 v13, v13, v21
  v_cvt_pk_bf16_f32 v16, v12, v13
  s_mov_b64 exec, -1
  v_cndmask_b32 v16, 0, v16, s[78:79]
  ds_bpermute_b32 v12, v7, v16
  ds_bpermute_b32 v13, v7, v16 offset:4
  ds_bpermute_b32 v14, v7, v16 offset:8
  v_mov_b32 v15, s62
  s_waitcnt lgkmcnt(0)
  s_mov_b32 exec_lo, 0xffff
  s_mov_b32 exec_hi, 0x0
  buffer_store_dwordx4 v[12:15], v8, s[48:51], s72 offen sc1
  s_mov_b64 exec, -1
  s_setprio 0
.ya47:
  s_xor_b32 s70, s70, 4160
.c244:
  s_lshl_b32 s72, s64, 2
  s_add_u32 s72, s72, 64
  s_sub_u32 s72, s72, 64
  s_add_u32 s62, s60, s72
  s_lshr_b32 s95, s72, 1
  s_and_b32 s95, s95, 2
  s_or_b32 s95, s95, 0
  s_mul_i32 s95, s95, 589824
  s_add_u32 s62, s62, 1
  s_or_b32 s62, s62, 0x80000000
  v_bfe_u32 v22, v0, 9, 1
  v_mul_u32_u24 v22, 393216, v22
  v_bfe_u32 v23, v0, 8, 1
  v_lshl_add_u32 v22, v23, 16, v22
  v_and_b32 v23, 0xf0, v0
  v_add_u32 v22, v22, v23
  s_lshl_b32 s73, s65, 13
  s_add_u32 s74, s73, 0
  buffer_load_dwordx4 v[28:31], v22, s[44:47], s74 offen
  s_add_u32 s74, s73, 131072
  buffer_load_dwordx4 v[32:35], v22, s[44:47], s74 offen
  s_add_u32 s74, s73, 262144
  buffer_load_dwordx4 v[36:39], v22, s[44:47], s74 offen
  s_xor_b32 s72, s70, 4160
  v_bfe_u32 v8, v0, 4, 6
  v_lshlrev_b32 v6, 3, v8
  v_add_u32 v6, s72, v6
  v_and_b32 v7, 7, v8
  v_lshlrev_b32 v7, 3, v7
  v_add_u32 v7, s72, v7
  v_lshlrev_b32 v8, 4, v8
  v_bfe_u32 v9, v0, 4, 6
  v_lshlrev_b32 v23, 4, v9
  s_lshl_b32 s76, s67, 5
  s_lshl_b32 s77, s65, 2
  s_add_u32 s76, s76, s77
  s_mul_i32 s76, s76, 256
  s_add_u32 s76, s76, s95
  s_add_u32 s76, s76, 262144
  v_bfe_u32 v9, v0, 4, 4
  v_bfe_u32 v27, v0, 9, 1
  v_mul_u32_u24 v27, 3, v27
  v_add_u32 v9, v9, v27
  v_bfe_u32 v27, v0, 8, 1
  v_lshl_or_b32 v9, v9, 1, v27
  v_lshlrev_b32 v27, 4, v9
  s_add_u32 s72, s64, 4
  s_sub_u32 s72, s72, 4
  s_mul_i32 s72, s72, 1280
  s_mul_i32 s73, s67, 160
  s_add_u32 s72, s72, s73
  s_lshl_b32 s73, s65, 1
  s_add_u32 s72, s72, s73
  buffer_load_ushort v22, v27, s[56:59], s72 offen
  v_bfe_u32 v25, v0, 4, 4
  v_mul_u32_u24 v25, 24, v25
  v_bfe_u32 v26, v0, 8, 1
  v_lshl_add_u32 v25, v26, 2, v25
  s_mul_i32 s72, s65, 384
  s_add_u32 s72, s72, s70
  v_add_u32 v25, s72, v25
  s_mul_i32 s72, s65, 40
  s_add_u32 s72, s72, s70
  v_lshlrev_b32 v26, 2, v9
  v_add_u32 v26, s72, v26
  buffer_load_dwordx4 v[10:13], v23, s[48:51], s76 offen sc1
  s_waitcnt vmcnt(0)
  v_cmp_eq_u32 vcc, s62, v13
  s_nop 0
  s_mov_b64 s[78:79], vcc
  s_cmp_eq_u64 s[78:79], exec
  s_nop 1
  s_cbranch_scc1 .ok48
  s_mov_b32 s94, 0
.re49:
  s_cmp_lg_u32 s71, 0
  s_cbranch_scc1 .ok48
  s_add_u32 s94, s94, 1
  s_cmp_gt_u32 s94, 200000
  s_cselect_b32 s71, 1, 0
  buffer_load_dwordx4 v[10:13], v23, s[48:51], s76 offen sc1
  s_waitcnt vmcnt(0)
  v_cmp_eq_u32 vcc, s62, v13
  s_nop 0
  s_mov_b64 s[78:79], vcc
  s_cmp_eq_u64 s[78:79], exec
  s_nop 1
  s_cbranch_scc0 .re49
.ok48:
  s_add_u32 s75, s68, 49152
  buffer_load_dwordx4 v[224:227], v0, s[28:31], s75 offen nt
  s_add_u32 s75, s68, 57344
  buffer_load_dwordx4 v[228:231], v0, s[28:31], s75 offen nt
  s_add_u32 s75, s68, 65536
  buffer_load_dwordx4 v[232:235], v0, s[28:31], s75 offen nt
  v_lshlrev_b32 v14, 16, v10
  v_and_b32 v15, 0xffff0000, v10
  v_lshlrev_b32 v16, 16, v11
  v_and_b32 v17, 0xffff0000, v11
  v_lshlrev_b32 v18, 16, v12
  v_and_b32 v19, 0xffff0000, v12
  v_permlane16_swap_b32 v14, v15
  v_permlane16_swap_b32 v16, v17
  v_permlane16_swap_b32 v18, v19
  v_add_f32 v14, v14, v15
  v_add_f32 v16, v16, v17
  v_add_f32 v18, v18, v19
  v_mov_b32 v20, v14
  v_mov_b32 v21, v16
  v_mov_b32 v24, v18
  v_permlane32_swap_b32 v14, v20
  v_permlane32_swap_b32 v16, v21
  v_permlane32_swap_b32 v18, v24
  v_add_f32 v14, v14, v20
  v_add_f32 v16, v16, v21
  v_add_f32 v18, v18, v24
  s_mov_b32 exec_lo, 0x3fff3fff
  s_mov_b32 exec_hi, 0x0
  ds_write_b32 v25, v14
  ds_write_b32 v25, v16 offset:8
  ds_write_b32 v25, v18 offset:16
  s_mov_b64 exec, -1
  s_waitcnt lgkmcnt(0)
  s_barrier
  ds_read_b32 v12, v26 offset:0
  ds_read_b32 v13, v26 offset:384
  ds_read_b32 v14, v26 offset:768
  ds_read_b32 v15, v26 offset:1152
  ds_read_b32 v16, v26 offset:1536
  ds_read_b32 v17, v26 offset:1920
  ds_read_b32 v18, v26 offset:2304
  ds_read_b32 v19, v26 offset:2688
  s_waitcnt lgkmcnt(4)
  v_add_f32 v12, v12, v13
  v_add_f32 v14, v14, v15
  s_waitcnt lgkmcnt(0)
  v_add_f32 v16, v16, v17
  v_add_f32 v18, v18, v19
  v_add_f32 v12, v12, v14
  v_add_f32 v16, v16, v18
  v_add_f32 v10, v12, v16
  v_lshlrev_b32 v22, 16, v22
  v_cvt_pk_bf16_f32 v10, v10, v10
  s_nop 0
  v_and_b32 v10, 0xffff0000, v10
  v_add_f32 v12, v22, v10
  v_cvt_pk_bf16_f32 v13, v12, v12
  s_nop 0
  v_and_b32 v12, 0xffff0000, v13
  s_xor_b32 s70, s70, 4160
  v_cmp_gt_u32 vcc, 10, v9
  v_mov_b32 v27, v12
  s_nop 0
  v_cndmask_b32 v24, 0, v13, vcc
  s_nop 1
  v_and_b32_dpp v10, v24, v2 row_newbcast:0 row_mask:0xf bank_mask:0xf
  v_and_b32_dpp v11, v24, v3 row_newbcast:0 row_mask:0xf bank_mask:0xf
  v_and_b32_dpp v12, v24, v4 row_newbcast:0 row_mask:0xf bank_mask:0xf
  v_and_b32_dpp v13, v24, v5 row_newbcast:0 row_mask:0xf bank_mask:0xf
  v_and_b32_dpp v14, v24, v2 row_newbcast:1 row_mask:0xf bank_mask:0xf
  v_and_b32_dpp v15, v24, v3 row_newbcast:1 row_mask:0xf bank_mask:0xf
  v_and_b32_dpp v16, v24, v4 row_newbcast:1 row_mask:0xf bank_mask:0xf
  v_and_b32_dpp v17, v24, v5 row_newbcast:1 row_mask:0xf bank_mask:0xf
  v_mfma_f32_16x16x32_bf16 v[20:23], v[28:31], v[10:13], 0
  v_and_b32_dpp v10, v24, v2 row_newbcast:2 row_mask:0xf bank_mask:0xf
  v_and_b32_dpp v11, v24, v3 row_newbcast:2 row_mask:0xf bank_mask:0xf
  v_and_b32_dpp v12, v24, v4 row_newbcast:2 row_mask:0xf bank_mask:0xf
  v_and_b32_dpp v13, v24, v5 row_newbcast:2 row_mask:0xf bank_mask:0xf
  v_mfma_f32_16x16x32_bf16 v[20:23], v[32:35], v[14:17], v[20:23]
  s_add_u32 s77, s64, 2
  s_and_b32 s77, s77, 1
  s_lshl_b32 s77, s77, 10
  s_add_u32 s77, s77, 158656
  s_lshl_b32 s72, s65, 6
)ASM"
R"ASM(
  s_add_u32 s77, s77, s72
  v_lshlrev_b32 v9, 2, v9
  v_add_u32 v9, s77, v9
  v_mfma_f32_16x16x32_bf16 v[20:23], v[36:39], v[10:13], v[20:23]
  s_mov_b32 exec_lo, 0x70007
  s_mov_b32 exec_hi, 0x30003
  ds_write_b32 v9, v27
  s_mov_b64 exec, -1
  v_and_b32 v9, 0xf0, v0
  v_lshlrev_b32 v9, 2, v9
  v_bfe_u32 v25, v0, 8, 2
  v_lshl_or_b32 v9, v25, 4, v9
  s_lshl_b32 s72, s65, 9
  s_add_u32 s72, s72, s70
  v_add_u32 v9, s72, v9
  s_mov_b32 s78, 0x70007
  s_mov_b32 s79, 0x30003
  v_cndmask_b32 v16, 0, v27, s[78:79]
  v_mul_f32 v17, v16, v16
  s_nop 0
  v_add_f32_dpp v16, v16, v16 quad_perm:[1,0,3,2] row_mask:0xf bank_mask:0xf
  v_add_f32_dpp v17, v17, v17 quad_perm:[1,0,3,2] row_mask:0xf bank_mask:0xf
  s_nop 0
  v_add_f32_dpp v16, v16, v16 quad_perm:[2,3,0,1] row_mask:0xf bank_mask:0xf
  v_add_f32_dpp v17, v17, v17 quad_perm:[2,3,0,1] row_mask:0xf bank_mask:0xf
  s_nop 1
  v_permlane16_swap_b32 v16, v17
  v_add_f32 v16, v16, v17
  v_mov_b32 v18, v16
  s_nop 1
  v_permlane32_swap_b32 v16, v18
  v_add_f32 v16, v16, v18
  s_lshl_b32 s72, s65, 3
  s_add_u32 s72, s72, s70
  v_bfe_u32 v19, v0, 8, 1
  v_lshlrev_b32 v19, 2, v19
  v_add_u32 v19, s72, v19
  s_mov_b32 exec_lo, 0x10001
  s_mov_b32 exec_hi, 0x0
  ds_write_b32 v19, v16 offset:4096
  s_mov_b64 exec, -1
  s_mov_b32 exec_lo, 0xff00ff
  s_mov_b32 exec_hi, 0xff00ff
  ds_write_b128 v9, v[20:23]
  s_mov_b64 exec, -1
  s_add_u32 s75, s68, 73728
  buffer_load_dwordx4 v[236:239], v0, s[28:31], s75 offen nt
  s_add_u32 s75, s68, 81920
  buffer_load_dwordx4 v[240:243], v0, s[28:31], s75 offen nt
  s_waitcnt lgkmcnt(0)
  s_barrier
  s_cmp_lg_u32 s65, 2
  s_cbranch_scc1 .ub50
  s_setprio 3
  ds_read_b64 v[12:13], v6 offset:0
  ds_read_b64 v[14:15], v6 offset:512
  ds_read_b64 v[16:17], v6 offset:1024
  ds_read_b64 v[18:19], v6 offset:1536
  ds_read_b64 v[20:21], v6 offset:2048
  ds_read_b64 v[22:23], v6 offset:2560
  ds_read_b64 v[24:25], v6 offset:3072
  ds_read_b64 v[26:27], v6 offset:3584
  ds_read_b64 v[28:29], v7 offset:4096
  s_add_u32 s72, s95, s98
  s_waitcnt lgkmcnt(5)
  v_add_f32 v12, v12, v14
  v_add_f32 v13, v13, v15
  v_add_f32 v16, v16, v18
  v_add_f32 v17, v17, v19
  s_waitcnt lgkmcnt(1)
  v_add_f32 v20, v20, v22
  v_add_f32 v21, v21, v23
  v_add_f32 v24, v24, v26
  v_add_f32 v25, v25, v27
  v_add_f32 v12, v12, v16
  v_add_f32 v13, v13, v17
  v_add_f32 v20, v20, v24
  v_add_f32 v21, v21, v25
  v_add_f32 v12, v12, v20
  v_add_f32 v13, v13, v21
  s_waitcnt lgkmcnt(0)
  v_add_f32_dpp v28, v28, v28 quad_perm:[1,0,3,2] row_mask:0xf bank_mask:0xf
  v_add_f32_dpp v29, v29, v29 quad_perm:[1,0,3,2] row_mask:0xf bank_mask:0xf
  s_nop 0
  v_add_f32_dpp v28, v28, v28 quad_perm:[2,3,0,1] row_mask:0xf bank_mask:0xf
  v_add_f32_dpp v29, v29, v29 quad_perm:[2,3,0,1] row_mask:0xf bank_mask:0xf
  s_nop 0
  v_add_f32_dpp v28, v28, v28 row_half_mirror row_mask:0xf bank_mask:0xf
  v_add_f32_dpp v29, v29, v29 row_half_mirror row_mask:0xf bank_mask:0xf
  v_cndmask_b32 v14, v28, v29, s[80:81]
  v_mov_b32 v15, s62
  buffer_store_dwordx4 v[12:15], v8, s[48:51], s72 offen sc1
  s_setprio 0
  s_nop 0
.ub50:
  s_xor_b32 s70, s70, 4160
  s_cmp_eq_u32 s64, 0
  s_cbranch_scc0 .c154
  s_branch .c255
.c154:
  s_cmp_lg_u32 s66, 0
  s_cbranch_scc1 .bf56
  s_lshl_b32 s72, s64, 2
  s_add_u32 s72, s72, 63
  s_sub_u32 s72, s72, 64
  s_add_u32 s62, s60, s72
  s_lshr_b32 s95, s72, 1
  s_and_b32 s95, s95, 2
  s_or_b32 s95, s95, 1
  s_mul_i32 s95, s95, 589824
  s_add_u32 s62, s62, 1
  s_or_b32 s62, s62, 0x80000000
  v_bfe_u32 v9, v0, 4, 6
  v_lshlrev_b32 v23, 4, v9
  s_lshl_b32 s76, s67, 5
  s_lshl_b32 s77, s65, 2
  s_add_u32 s76, s76, s77
  s_mul_i32 s76, s76, 256
  s_add_u32 s76, s76, s95
  s_add_u32 s76, s76, 262144
  v_bfe_u32 v9, v0, 4, 4
  v_bfe_u32 v27, v0, 9, 1
  v_mul_u32_u24 v27, 3, v27
  v_add_u32 v9, v9, v27
  v_bfe_u32 v27, v0, 8, 1
  v_lshl_or_b32 v9, v9, 1, v27
  s_add_u32 s77, s64, 1
  s_and_b32 s77, s77, 1
  s_lshl_b32 s77, s77, 10
  s_add_u32 s77, s77, 158656
  s_lshl_b32 s72, s65, 6
  s_add_u32 s77, s77, s72
  v_lshlrev_b32 v27, 2, v9
  v_add_u32 v27, s77, v27
  ds_read_b32 v22, v27
  v_bfe_u32 v25, v0, 4, 4
  v_mul_u32_u24 v25, 24, v25
  v_bfe_u32 v26, v0, 8, 1
  v_lshl_add_u32 v25, v26, 2, v25
  s_mul_i32 s72, s65, 384
  s_add_u32 s72, s72, s70
  v_add_u32 v25, s72, v25
  s_mul_i32 s72, s65, 40
  s_add_u32 s72, s72, s70
  v_lshlrev_b32 v26, 2, v9
  v_add_u32 v26, s72, v26
  buffer_load_dwordx4 v[10:13], v23, s[48:51], s76 offen sc1
  s_waitcnt vmcnt(0)
  v_cmp_eq_u32 vcc, s62, v13
  s_nop 0
  s_mov_b64 s[78:79], vcc
  s_cmp_eq_u64 s[78:79], exec
  s_nop 1
  s_cbranch_scc1 .ok58
  s_mov_b32 s94, 0
.re59:
  s_cmp_lg_u32 s71, 0
  s_cbranch_scc1 .ok58
  s_add_u32 s94, s94, 1
  s_cmp_gt_u32 s94, 200000
  s_cselect_b32 s71, 1, 0
  buffer_load_dwordx4 v[10:13], v23, s[48:51], s76 offen sc1
  s_waitcnt vmcnt(0)
  v_cmp_eq_u32 vcc, s62, v13
  s_nop 0
  s_mov_b64 s[78:79], vcc
  s_cmp_eq_u64 s[78:79], exec
  s_nop 1
  s_cbranch_scc0 .re59
.ok58:
  v_lshlrev_b32 v14, 16, v10
  v_and_b32 v15, 0xffff0000, v10
  v_lshlrev_b32 v16, 16, v11
  v_and_b32 v17, 0xffff0000, v11
  v_lshlrev_b32 v18, 16, v12
  v_and_b32 v19, 0xffff0000, v12
  v_permlane16_swap_b32 v14, v15
  v_permlane16_swap_b32 v16, v17
  v_permlane16_swap_b32 v18, v19
  v_add_f32 v14, v14, v15
  v_add_f32 v16, v16, v17
  v_add_f32 v18, v18, v19
  v_mov_b32 v20, v14
  v_mov_b32 v21, v16
  v_mov_b32 v24, v18
  v_permlane32_swap_b32 v14, v20
  v_permlane32_swap_b32 v16, v21
  v_permlane32_swap_b32 v18, v24
  v_add_f32 v14, v14, v20
  v_add_f32 v16, v16, v21
  v_add_f32 v18, v18, v24
  s_mov_b32 exec_lo, 0x3fff3fff
  s_mov_b32 exec_hi, 0x0
  ds_write_b32 v25, v14
  ds_write_b32 v25, v16 offset:8
  ds_write_b32 v25, v18 offset:16
  s_mov_b64 exec, -1
  s_waitcnt lgkmcnt(0)
  s_barrier
  ds_read_b32 v12, v26 offset:0
)ASM"
R"ASM(
  ds_read_b32 v13, v26 offset:384
  ds_read_b32 v14, v26 offset:768
  ds_read_b32 v15, v26 offset:1152
  ds_read_b32 v16, v26 offset:1536
  ds_read_b32 v17, v26 offset:1920
  ds_read_b32 v18, v26 offset:2304
  ds_read_b32 v19, v26 offset:2688
  s_waitcnt lgkmcnt(4)
  v_add_f32 v12, v12, v13
  v_add_f32 v14, v14, v15
  s_waitcnt lgkmcnt(0)
  v_add_f32 v16, v16, v17
  v_add_f32 v18, v18, v19
  v_add_f32 v12, v12, v14
  v_add_f32 v16, v16, v18
  v_add_f32 v10, v12, v16
  v_cvt_pk_bf16_f32 v10, v10, v10
  s_nop 0
  v_and_b32 v10, 0xffff0000, v10
  v_add_f32 v12, v22, v10
  v_cvt_pk_bf16_f32 v13, v12, v12
  s_nop 0
  v_and_b32 v12, 0xffff0000, v13
  v_lshlrev_b32 v9, 4, v9
  s_add_u32 s72, s64, 3
  s_sub_u32 s72, s72, 4
  s_mul_i32 s72, s72, 1280
  s_mul_i32 s73, s67, 160
  s_add_u32 s72, s72, s73
  s_lshl_b32 s73, s65, 1
  s_add_u32 s72, s72, s73
  s_mov_b32 exec_lo, 0x70007
  s_mov_b32 exec_hi, 0x30003
  buffer_store_short v13, v9, s[52:55], s72 offen
  s_mov_b64 exec, -1
  s_xor_b32 s70, s70, 4160
.bf56:
.c255:
  s_lshl_b32 s72, s64, 2
  s_add_u32 s72, s72, 64
  s_sub_u32 s72, s72, 64
  s_add_u32 s62, s60, s72
  s_lshr_b32 s95, s72, 1
  s_and_b32 s95, s95, 2
  s_or_b32 s95, s95, 0
  s_mul_i32 s95, s95, 589824
  s_add_u32 s62, s62, 1
  s_or_b32 s62, s62, 0x80000000
  v_bfe_u32 v9, v0, 4, 6
  v_lshrrev_b32 v20, 3, v9
  v_mul_u32_u24 v20, 1024, v20
  v_and_b32 v9, 7, v9
  v_lshl_add_u32 v20, v9, 4, v20
  s_mul_i32 s76, s66, 8192
  s_add_u32 s76, s76, s95
  s_add_u32 s76, s76, 327680
  s_lshl_b32 s73, s65, 7
  s_add_u32 s76, s76, s73
  v_bfe_u32 v18, v0, 8, 1
  v_lshl_or_b32 v19, v9, 1, v18
  v_lshlrev_b32 v19, 2, v19
  s_lshl_b32 s73, s65, 6
  s_add_u32 s73, s73, 157632
  v_add_u32 v19, s73, v19
  ds_read_b32 v22, v19
  v_lshl_or_b32 v18, v9, 1, v18
  v_lshlrev_b32 v18, 4, v18
  s_lshl_b32 s73, s65, 1
  s_add_u32 s73, s73, 155840
  v_add_u32 v18, s73, v18
  s_sleep 8
  buffer_load_dwordx4 v[10:13], v20, s[48:51], s76 offen sc1
  s_waitcnt vmcnt(0)
  v_cmp_eq_u32 vcc, s62, v13
  s_nop 0
  s_mov_b64 s[78:79], vcc
  s_cmp_eq_u64 s[78:79], exec
  s_nop 1
  s_cbranch_scc1 .ok60
  s_mov_b32 s94, 0
.re61:
  s_cmp_lg_u32 s71, 0
  s_cbranch_scc1 .ok60
  s_add_u32 s94, s94, 1
  s_cmp_gt_u32 s94, 200000
  s_cselect_b32 s71, 1, 0
  buffer_load_dwordx4 v[10:13], v20, s[48:51], s76 offen sc1
  s_waitcnt vmcnt(0)
  v_cmp_eq_u32 vcc, s62, v13
  s_nop 0
  s_mov_b64 s[78:79], vcc
  s_cmp_eq_u64 s[78:79], exec
  s_nop 1
  s_cbranch_scc0 .re61
.ok60:
  s_add_u32 s75, s68, 90112
  buffer_load_dwordx4 v[244:247], v0, s[28:31], s75 offen nt
  s_add_u32 s75, s68, 98304
  buffer_load_dwordx4 v[248:251], v0, s[28:31], s75 offen nt
  s_add_u32 s75, s68, 106496
  buffer_load_dwordx4 v[252:255], v0, s[28:31], s75 offen nt
  v_add_f32_dpp v12, v12, v12 row_ror:8 row_mask:0xf bank_mask:0xf
  v_add_f32_dpp v10, v10, v10 row_ror:8 row_mask:0xf bank_mask:0xf
  v_add_f32_dpp v11, v11, v11 row_ror:8 row_mask:0xf bank_mask:0xf
  v_mov_b32 v16, v12
  s_nop 1
  v_permlane16_swap_b32 v12, v16
  v_permlane16_swap_b32 v10, v11
  v_add_f32 v12, v12, v16
  v_add_f32 v10, v10, v11
  v_mov_b32 v16, v12
  v_mov_b32 v17, v10
  s_nop 0
  v_permlane32_swap_b32 v12, v16
  v_permlane32_swap_b32 v10, v17
  v_add_f32 v12, v12, v16
  v_add_f32 v10, v10, v17
  s_nop 0
  v_mov_b32_dpp v14, v12 quad_perm:[0,0,2,2] row_mask:0xf bank_mask:0xf
  v_mov_b32_dpp v15, v12 quad_perm:[1,1,3,3] row_mask:0xf bank_mask:0xf
  v_mov_b32 v16, 0x3727c5ac
  v_mul_f32 v14, 0x3acccccd, v14
  v_mul_f32 v15, 0x3acccccd, v15
  v_fma_f32 v15, -v14, v14, v15
  v_max_f32 v15, 0, v15
  v_add_f32 v15, v15, v16
  v_rsq_f32 v15, v15
  s_waitcnt lgkmcnt(0)
  v_fma_f32 v10, -v14, v22, v10
  v_mul_f32 v10, v10, v15
  v_cvt_pk_bf16_f32 v10, v10, v10
  s_mov_b32 exec_lo, 0xff00ff
  s_mov_b32 exec_hi, 0x0
  ds_write_b16 v18, v10
  s_mov_b64 exec, -1
  s_lshl_b32 s74, s65, 10
  s_add_u32 s74, s74, 131072
  s_mov_b32 m0, s74
  s_add_u32 s75, s68, 114688
  buffer_load_dwordx4 v0, s[28:31], s75 offen nt lds
  s_lshl_b32 s74, s65, 10
  s_add_u32 s74, s74, 139264
  s_mov_b32 m0, s74
  s_add_u32 s75, s68, 122880
  buffer_load_dwordx4 v0, s[28:31], s75 offen nt lds
  s_waitcnt lgkmcnt(0)
  s_barrier
  s_waitcnt vmcnt(22)
  s_mov_b32 s76, 155840
  v_and_b32 v9, 0xff, v1
  v_add_u32 v9, s76, v9
  v_mov_b32 v22, 155776
  v_cndmask_b32 v23, v22, v9, s[84:85]
  ds_read_b128 v[24:27], v23
  s_nop 0
  v_cndmask_b32 v23, v22, v9, s[86:87]
  ds_read_b128 v[28:31], v23
  s_nop 0
  v_cndmask_b32 v23, v22, v9, s[88:89]
  ds_read_b128 v[32:35], v23
  s_nop 0
  v_cndmask_b32 v23, v22, v9, s[90:91]
  ds_read_b128 v[36:39], v23
  s_waitcnt lgkmcnt(0)
  v_mfma_f32_16x16x32_bf16 v[10:13], v[136:139], v[24:27], 0
  v_mfma_f32_16x16x32_bf16 v[10:13], v[140:143], v[28:31], v[10:13]
  v_mfma_f32_16x16x32_bf16 v[10:13], v[144:147], v[32:35], v[10:13]
  v_mfma_f32_16x16x32_bf16 v[10:13], v[148:151], v[36:39], v[10:13]
  v_mfma_f32_16x16x32_bf16 v[14:17], v[168:171], v[24:27], 0
  v_mfma_f32_16x16x32_bf16 v[14:17], v[172:175], v[28:31], v[14:17]
  v_mfma_f32_16x16x32_bf16 v[14:17], v[176:179], v[32:35], v[14:17]
  v_mfma_f32_16x16x32_bf16 v[14:17], v[180:183], v[36:39], v[14:17]
  s_nop 3
  v_cndmask_b32 v22, v10, v11, s[80:81]
  v_cndmask_b32 v23, v12, v13, s[80:81]
  v_mfma_f32_16x16x32_bf16 v[10:13], v[152:155], v[24:27], 0
  v_mfma_f32_16x16x32_bf16 v[10:13], v[156:159], v[28:31], v[10:13]
  v_mfma_f32_16x16x32_bf16 v[10:13], v[160:163], v[32:35], v[10:13]
  v_mfma_f32_16x16x32_bf16 v[10:13], v[164:167], v[36:39], v[10:13]
  v_cndmask_b32 v14, v14, v15, s[80:81]
  v_cndmask_b32 v16, v16, v17, s[80:81]
  v_cndmask_b32 v15, v22, v16, s[82:83]
  v_cndmask_b32 v17, v14, v23, s[82:83]
  s_nop 1
  v_add_f32_dpp v23, v17, v15 quad_perm:[2,3,0,1] row_mask:0xf bank_mask:0xf
  v_mfma_f32_16x16x32_bf16 v[14:17], v[184:187], v[24:27], 0
  v_mfma_f32_16x16x32_bf16 v[14:17], v[188:191], v[28:31], v[14:17]
  v_mfma_f32_16x16x32_bf16 v[14:17], v[192:195], v[32:35], v[14:17]
  v_mfma_f32_16x16x32_bf16 v[14:17], v[196:199], v[36:39], v[14:17]
  v_cndmask_b32 v22, v10, v11, s[80:81]
  v_cndmask_b32 v9, v12, v13, s[80:81]
  s_nop 5
  v_cndmask_b32 v14, v14, v15, s[80:81]
  v_cndmask_b32 v16, v16, v17, s[80:81]
  v_cndmask_b32 v15, v22, v16, s[82:83]
  v_cndmask_b32 v17, v14, v9, s[82:83]
  s_nop 1
  v_add_f32_dpp v22, v17, v15 quad_perm:[2,3,0,1] row_mask:0xf bank_mask:0xf
  v_cndmask_b32 v10, v23, v22, s[80:81]
  v_cndmask_b32 v11, v22, v23, s[80:81]
)ASM"
R"ASM(
  s_nop 1
  v_add_f32_dpp v12, v11, v10 quad_perm:[1,0,3,2] row_mask:0xf bank_mask:0xf
  s_nop 1
  v_max_f32_dpp v13, v12, v12 row_ror:8 row_mask:0xf bank_mask:0xf
  s_nop 1
  v_max_f32_dpp v13, v13, v13 row_ror:4 row_mask:0xf bank_mask:0xf
  s_nop 1
  v_max_f32_dpp v13, v13, v13 quad_perm:[2,3,0,1] row_mask:0xf bank_mask:0xf
  s_nop 1
  v_max_f32_dpp v13, v13, v13 quad_perm:[1,0,3,2] row_mask:0xf bank_mask:0xf
  v_mov_b32 v14, v13
  s_nop 1
  v_permlane16_swap_b32 v13, v14
  v_max_f32 v13, v13, v14
  v_mov_b32 v14, v13
  s_nop 1
  v_permlane32_swap_b32 v13, v14
  v_max_f32 v13, v13, v14
  v_mul_f32 v37, s92, v13
  v_fma_f32 v14, v12, s92, -v37
  v_exp_f32 v38, v14
  s_nop 0
  v_cvt_pk_bf16_f32 v36, v38, v38
  s_nop 1
  v_and_b32_dpp v10, v36, v2 row_newbcast:0 row_mask:0xf bank_mask:0xf
  v_and_b32_dpp v11, v36, v3 row_newbcast:0 row_mask:0xf bank_mask:0xf
  v_and_b32_dpp v12, v36, v4 row_newbcast:0 row_mask:0xf bank_mask:0xf
  v_and_b32_dpp v13, v36, v5 row_newbcast:0 row_mask:0xf bank_mask:0xf
  s_waitcnt vmcnt(21)
  v_and_b32_dpp v14, v36, v2 row_newbcast:4 row_mask:0xf bank_mask:0xf
  v_and_b32_dpp v15, v36, v3 row_newbcast:4 row_mask:0xf bank_mask:0xf
  v_and_b32_dpp v16, v36, v4 row_newbcast:4 row_mask:0xf bank_mask:0xf
  v_and_b32_dpp v17, v36, v5 row_newbcast:4 row_mask:0xf bank_mask:0xf
  v_mfma_f32_16x16x32_bf16 v[18:21], v[200:203], v[10:13], 0
  s_waitcnt vmcnt(20)
  v_and_b32_dpp v10, v36, v2 row_newbcast:8 row_mask:0xf bank_mask:0xf
  v_and_b32_dpp v11, v36, v3 row_newbcast:8 row_mask:0xf bank_mask:0xf
  v_and_b32_dpp v12, v36, v4 row_newbcast:8 row_mask:0xf bank_mask:0xf
  v_and_b32_dpp v13, v36, v5 row_newbcast:8 row_mask:0xf bank_mask:0xf
  v_mfma_f32_16x16x32_bf16 v[18:21], v[204:207], v[14:17], v[18:21]
  s_waitcnt vmcnt(19)
  v_and_b32_dpp v14, v36, v2 row_newbcast:12 row_mask:0xf bank_mask:0xf
  v_and_b32_dpp v15, v36, v3 row_newbcast:12 row_mask:0xf bank_mask:0xf
  v_and_b32_dpp v16, v36, v4 row_newbcast:12 row_mask:0xf bank_mask:0xf
  v_and_b32_dpp v17, v36, v5 row_newbcast:12 row_mask:0xf bank_mask:0xf
  v_mfma_f32_16x16x32_bf16 v[18:21], v[208:211], v[10:13], v[18:21]
  s_waitcnt vmcnt(18)
  v_and_b32_dpp v10, v36, v2 row_newbcast:1 row_mask:0xf bank_mask:0xf
  v_and_b32_dpp v11, v36, v3 row_newbcast:1 row_mask:0xf bank_mask:0xf
  v_and_b32_dpp v12, v36, v4 row_newbcast:1 row_mask:0xf bank_mask:0xf
  v_and_b32_dpp v13, v36, v5 row_newbcast:1 row_mask:0xf bank_mask:0xf
  v_mfma_f32_16x16x32_bf16 v[18:21], v[212:215], v[14:17], v[18:21]
  s_waitcnt vmcnt(17)
  v_and_b32_dpp v14, v36, v2 row_newbcast:5 row_mask:0xf bank_mask:0xf
  v_and_b32_dpp v15, v36, v3 row_newbcast:5 row_mask:0xf bank_mask:0xf
  v_and_b32_dpp v16, v36, v4 row_newbcast:5 row_mask:0xf bank_mask:0xf
  v_and_b32_dpp v17, v36, v5 row_newbcast:5 row_mask:0xf bank_mask:0xf
  v_mfma_f32_16x16x32_bf16 v[18:21], v[216:219], v[10:13], v[18:21]
  s_waitcnt vmcnt(16)
  v_and_b32_dpp v10, v36, v2 row_newbcast:9 row_mask:0xf bank_mask:0xf
  v_and_b32_dpp v11, v36, v3 row_newbcast:9 row_mask:0xf bank_mask:0xf
  v_and_b32_dpp v12, v36, v4 row_newbcast:9 row_mask:0xf bank_mask:0xf
  v_and_b32_dpp v13, v36, v5 row_newbcast:9 row_mask:0xf bank_mask:0xf
  v_mfma_f32_16x16x32_bf16 v[18:21], v[220:223], v[14:17], v[18:21]
  s_waitcnt vmcnt(10)
  v_and_b32_dpp v14, v36, v2 row_newbcast:13 row_mask:0xf bank_mask:0xf
  v_and_b32_dpp v15, v36, v3 row_newbcast:13 row_mask:0xf bank_mask:0xf
  v_and_b32_dpp v16, v36, v4 row_newbcast:13 row_mask:0xf bank_mask:0xf
  v_and_b32_dpp v17, v36, v5 row_newbcast:13 row_mask:0xf bank_mask:0xf
  v_mfma_f32_16x16x32_bf16 v[18:21], v[224:227], v[10:13], v[18:21]
  s_waitcnt vmcnt(9)
  v_and_b32_dpp v10, v36, v2 row_newbcast:2 row_mask:0xf bank_mask:0xf
  v_and_b32_dpp v11, v36, v3 row_newbcast:2 row_mask:0xf bank_mask:0xf
  v_and_b32_dpp v12, v36, v4 row_newbcast:2 row_mask:0xf bank_mask:0xf
  v_and_b32_dpp v13, v36, v5 row_newbcast:2 row_mask:0xf bank_mask:0xf
  v_mfma_f32_16x16x32_bf16 v[18:21], v[228:231], v[14:17], v[18:21]
  s_waitcnt vmcnt(8)
  v_and_b32_dpp v14, v36, v2 row_newbcast:6 row_mask:0xf bank_mask:0xf
  v_and_b32_dpp v15, v36, v3 row_newbcast:6 row_mask:0xf bank_mask:0xf
  v_and_b32_dpp v16, v36, v4 row_newbcast:6 row_mask:0xf bank_mask:0xf
  v_and_b32_dpp v17, v36, v5 row_newbcast:6 row_mask:0xf bank_mask:0xf
  v_mfma_f32_16x16x32_bf16 v[18:21], v[232:235], v[10:13], v[18:21]
  s_waitcnt vmcnt(7)
  v_and_b32_dpp v10, v36, v2 row_newbcast:10 row_mask:0xf bank_mask:0xf
  v_and_b32_dpp v11, v36, v3 row_newbcast:10 row_mask:0xf bank_mask:0xf
  v_and_b32_dpp v12, v36, v4 row_newbcast:10 row_mask:0xf bank_mask:0xf
  v_and_b32_dpp v13, v36, v5 row_newbcast:10 row_mask:0xf bank_mask:0xf
  v_mfma_f32_16x16x32_bf16 v[18:21], v[236:239], v[14:17], v[18:21]
  s_waitcnt vmcnt(6)
  v_and_b32_dpp v14, v36, v2 row_newbcast:14 row_mask:0xf bank_mask:0xf
  v_and_b32_dpp v15, v36, v3 row_newbcast:14 row_mask:0xf bank_mask:0xf
  v_and_b32_dpp v16, v36, v4 row_newbcast:14 row_mask:0xf bank_mask:0xf
  v_and_b32_dpp v17, v36, v5 row_newbcast:14 row_mask:0xf bank_mask:0xf
  v_mfma_f32_16x16x32_bf16 v[18:21], v[240:243], v[10:13], v[18:21]
  s_waitcnt vmcnt(4)
  v_and_b32_dpp v10, v36, v2 row_newbcast:3 row_mask:0xf bank_mask:0xf
  v_and_b32_dpp v11, v36, v3 row_newbcast:3 row_mask:0xf bank_mask:0xf
  v_and_b32_dpp v12, v36, v4 row_newbcast:3 row_mask:0xf bank_mask:0xf
  v_and_b32_dpp v13, v36, v5 row_newbcast:3 row_mask:0xf bank_mask:0xf
  v_mfma_f32_16x16x32_bf16 v[18:21], v[244:247], v[14:17], v[18:21]
  s_waitcnt vmcnt(3)
  v_and_b32_dpp v14, v36, v2 row_newbcast:7 row_mask:0xf bank_mask:0xf
  v_and_b32_dpp v15, v36, v3 row_newbcast:7 row_mask:0xf bank_mask:0xf
  v_and_b32_dpp v16, v36, v4 row_newbcast:7 row_mask:0xf bank_mask:0xf
  v_and_b32_dpp v17, v36, v5 row_newbcast:7 row_mask:0xf bank_mask:0xf
  v_mfma_f32_16x16x32_bf16 v[18:21], v[248:251], v[10:13], v[18:21]
  s_waitcnt vmcnt(2)
  v_and_b32_dpp v10, v36, v2 row_newbcast:11 row_mask:0xf bank_mask:0xf
  v_and_b32_dpp v11, v36, v3 row_newbcast:11 row_mask:0xf bank_mask:0xf
  v_and_b32_dpp v12, v36, v4 row_newbcast:11 row_mask:0xf bank_mask:0xf
  v_and_b32_dpp v13, v36, v5 row_newbcast:11 row_mask:0xf bank_mask:0xf
  v_mfma_f32_16x16x32_bf16 v[18:21], v[252:255], v[14:17], v[18:21]
  s_waitcnt vmcnt(1)
  v_add_u32 v9, 131072, v0
  ds_read_b128 v[28:31], v9
  s_waitcnt lgkmcnt(0)
  v_and_b32_dpp v14, v36, v2 row_newbcast:15 row_mask:0xf bank_mask:0xf
  v_and_b32_dpp v15, v36, v3 row_newbcast:15 row_mask:0xf bank_mask:0xf
  v_and_b32_dpp v16, v36, v4 row_newbcast:15 row_mask:0xf bank_mask:0xf
  v_and_b32_dpp v17, v36, v5 row_newbcast:15 row_mask:0xf bank_mask:0xf
  v_mfma_f32_16x16x32_bf16 v[18:21], v[28:31], v[10:13], v[18:21]
  s_waitcnt vmcnt(0)
  v_add_u32 v9, 139264, v0
  ds_read_b128 v[32:35], v9
  s_waitcnt lgkmcnt(0)
  v_mfma_f32_16x16x32_bf16 v[18:21], v[32:35], v[14:17], v[18:21]
  v_add_f32_dpp v22, v38, v38 row_ror:8 row_mask:0xf bank_mask:0xf
  s_nop 1
  v_add_f32_dpp v22, v22, v22 row_ror:4 row_mask:0xf bank_mask:0xf
  s_nop 1
  v_add_f32_dpp v22, v22, v22 quad_perm:[2,3,0,1] row_mask:0xf bank_mask:0xf
  s_nop 1
  v_add_f32_dpp v22, v22, v22 quad_perm:[1,0,3,2] row_mask:0xf bank_mask:0xf
  v_mov_b32 v23, v22
  s_nop 1
  v_permlane16_swap_b32 v22, v23
  v_add_f32 v22, v22, v23
  v_mov_b32 v23, v22
  s_nop 1
  v_permlane32_swap_b32 v22, v23
  v_add_f32 v22, v22, v23
  v_and_b32 v9, 0xf0, v0
  v_lshlrev_b32 v9, 2, v9
  v_bfe_u32 v10, v0, 8, 2
  v_lshl_or_b32 v9, v10, 4, v9
  s_lshl_b32 s72, s65, 9
  s_add_u32 s72, s72, s70
  v_add_u32 v9, s72, v9
  s_mov_b32 exec_lo, 0xff00ff
  s_mov_b32 exec_hi, 0xff00ff
  ds_write_b128 v9, v[18:21]
  s_mov_b32 exec_lo, 0x1
  s_mov_b32 exec_hi, 0x0
  s_lshl_b32 s72, s65, 3
  s_add_u32 s72, s72, s70
  v_mov_b32 v9, s72
  v_mov_b32 v23, v22
  v_mov_b32 v22, v37
  ds_write_b64 v9, v[22:23] offset:4096
  s_mov_b64 exec, -1
  v_bfe_u32 v9, v0, 4, 6
  v_lshlrev_b32 v28, 3, v9
  v_add_u32 v28, s70, v28
  v_and_b32 v29, 7, v9
  v_lshlrev_b32 v29, 3, v29
  v_add_u32 v29, s70, v29
  v_lshlrev_b32 v9, 4, v9
  s_lshl_b32 s72, s64, 2
  s_add_u32 s72, s72, 65
  s_sub_u32 s72, s72, 64
  s_add_u32 s62, s60, s72
  s_lshr_b32 s95, s72, 1
  s_and_b32 s95, s95, 2
  s_or_b32 s95, s95, 1
  s_mul_i32 s95, s95, 589824
  s_add_u32 s62, s62, 1
  s_or_b32 s62, s62, 0x80000000
  s_add_u32 s72, s95, s96
  s_waitcnt lgkmcnt(0)
  s_barrier
  s_cmp_lg_u32 s65, 0
  s_cbranch_scc1 .mg62
  s_setprio 3
  ds_read_b64 v[26:27], v29 offset:4096
  ds_read_b64 v[10:11], v28 offset:0
  ds_read_b64 v[12:13], v28 offset:512
  ds_read_b64 v[14:15], v28 offset:1024
  ds_read_b64 v[16:17], v28 offset:1536
  ds_read_b64 v[18:19], v28 offset:2048
  ds_read_b64 v[20:21], v28 offset:2560
  ds_read_b64 v[22:23], v28 offset:3072
  ds_read_b64 v[24:25], v28 offset:3584
  s_waitcnt lgkmcnt(8)
  v_mov_b32 v30, v26
  s_nop 1
  v_max_f32_dpp v30, v30, v30 quad_perm:[1,0,3,2] row_mask:0xf bank_mask:0xf
  s_nop 1
  v_max_f32_dpp v30, v30, v30 quad_perm:[2,3,0,1] row_mask:0xf bank_mask:0xf
  s_nop 1
  v_max_f32_dpp v30, v30, v30 row_half_mirror row_mask:0xf bank_mask:0xf
)ASM"
R"ASM(
  v_sub_f32 v31, v26, v30
  v_exp_f32 v31, v31
  s_nop 0
  v_mul_f32 v32, v31, v27
  v_mov_b32 v36, 0
  v_mov_b32 v37, 0
  v_mov_b32 v39, s62
  v_add_f32_dpp v32, v32, v32 quad_perm:[1,0,3,2] row_mask:0xf bank_mask:0xf
  s_nop 1
  v_add_f32_dpp v32, v32, v32 quad_perm:[2,3,0,1] row_mask:0xf bank_mask:0xf
  s_nop 1
  v_add_f32_dpp v32, v32, v32 row_half_mirror row_mask:0xf bank_mask:0xf
  s_waitcnt lgkmcnt(0)
  v_fmac_f32_dpp v36, v31, v10 row_newbcast:0 row_mask:0xf bank_mask:0xf
  v_fmac_f32_dpp v37, v31, v11 row_newbcast:0 row_mask:0xf bank_mask:0xf
  s_nop 0
  v_fmac_f32_dpp v36, v31, v12 row_newbcast:1 row_mask:0xf bank_mask:0xf
  v_fmac_f32_dpp v37, v31, v13 row_newbcast:1 row_mask:0xf bank_mask:0xf
  s_nop 0
  v_fmac_f32_dpp v36, v31, v14 row_newbcast:2 row_mask:0xf bank_mask:0xf
  v_fmac_f32_dpp v37, v31, v15 row_newbcast:2 row_mask:0xf bank_mask:0xf
  s_nop 0
  v_fmac_f32_dpp v36, v31, v16 row_newbcast:3 row_mask:0xf bank_mask:0xf
  v_fmac_f32_dpp v37, v31, v17 row_newbcast:3 row_mask:0xf bank_mask:0xf
  s_nop 0
  v_fmac_f32_dpp v36, v31, v18 row_newbcast:4 row_mask:0xf bank_mask:0xf
  v_fmac_f32_dpp v37, v31, v19 row_newbcast:4 row_mask:0xf bank_mask:0xf
  s_nop 0
  v_fmac_f32_dpp v36, v31, v20 row_newbcast:5 row_mask:0xf bank_mask:0xf
  v_fmac_f32_dpp v37, v31, v21 row_newbcast:5 row_mask:0xf bank_mask:0xf
  s_nop 0
  v_fmac_f32_dpp v36, v31, v22 row_newbcast:6 row_mask:0xf bank_mask:0xf
  v_fmac_f32_dpp v37, v31, v23 row_newbcast:6 row_mask:0xf bank_mask:0xf
  s_nop 0
  v_fmac_f32_dpp v36, v31, v24 row_newbcast:7 row_mask:0xf bank_mask:0xf
  v_fmac_f32_dpp v37, v31, v25 row_newbcast:7 row_mask:0xf bank_mask:0xf
  v_cndmask_b32 v38, v30, v32, s[80:81]
  buffer_store_dwordx4 v[36:39], v9, s[48:51], s72 offen sc1
  s_setprio 0
  s_nop 0
.mg62:
  s_xor_b32 s70, s70, 4160
  s_lshl_b32 s72, s64, 2
  s_add_u32 s72, s72, 65
  s_sub_u32 s72, s72, 64
  s_add_u32 s62, s60, s72
  s_lshr_b32 s95, s72, 1
  s_and_b32 s95, s95, 2
  s_or_b32 s95, s95, 1
  s_mul_i32 s95, s95, 589824
  s_add_u32 s62, s62, 1
  s_or_b32 s62, s62, 0x80000000
  v_bfe_u32 v19, v0, 8, 1
  v_mul_u32_u24 v19, 10240, v19
  v_bfe_u32 v18, v0, 9, 1
  v_mul_u32_u24 v18, 81920, v18
  v_add_u32 v19, v19, v18
  v_and_b32 v18, 0xf0, v0
  v_add_u32 v19, v19, v18
  s_mul_i32 s73, s65, 1280
  s_mov_b32 exec_lo, 0x3ff03ff
  s_mov_b32 exec_hi, 0x3ff03ff
  s_add_u32 s74, s73, 0
  buffer_load_dwordx4 v[24:27], v19, s[36:39], s74 offen
  s_add_u32 s74, s73, 20480
  buffer_load_dwordx4 v[28:31], v19, s[36:39], s74 offen
  s_add_u32 s74, s73, 40960
  buffer_load_dwordx4 v[32:35], v19, s[36:39], s74 offen
  s_add_u32 s74, s73, 61440
  buffer_load_dwordx4 v[36:39], v19, s[36:39], s74 offen
  s_mov_b64 exec, -1
  v_bfe_u32 v8, v0, 4, 6
  v_mul_u32_u24 v6, 52, v8
  v_lshrrev_b32 v6, 8, v6
  v_mul_u32_u24 v7, 5, v6
  v_sub_u32 v7, v8, v7
  v_lshlrev_b32 v6, 6, v6
  v_lshl_add_u32 v6, v7, 3, v6
  v_add_u32 v6, s70, v6
  v_mul_u32_u24 v7, 12, v8
  v_lshlrev_b32 v8, 4, v8
  v_bfe_u32 v9, v0, 4, 6
  v_lshrrev_b32 v20, 3, v9
  v_mul_u32_u24 v20, 1024, v20
  v_and_b32 v9, 7, v9
  v_lshl_add_u32 v20, v9, 4, v20
  s_mul_i32 s76, s66, 8192
  s_add_u32 s76, s76, s95
  s_lshl_b32 s73, s65, 7
  s_add_u32 s76, s76, s73
  s_sleep 20
  buffer_load_dwordx4 v[10:13], v20, s[48:51], s76 offen sc1
  s_waitcnt vmcnt(0)
  v_cmp_eq_u32 vcc, s62, v13
  s_nop 0
  s_mov_b64 s[78:79], vcc
  s_cmp_eq_u64 s[78:79], exec
  s_nop 1
  s_cbranch_scc1 .ok63
  s_mov_b32 s94, 0
.re64:
  s_cmp_lg_u32 s71, 0
  s_cbranch_scc1 .ok63
  s_add_u32 s94, s94, 1
  s_cmp_gt_u32 s94, 200000
  s_cselect_b32 s71, 1, 0
  buffer_load_dwordx4 v[10:13], v20, s[48:51], s76 offen sc1
  s_waitcnt vmcnt(0)
  v_cmp_eq_u32 vcc, s62, v13
  s_nop 0
  s_mov_b64 s[78:79], vcc
  s_cmp_eq_u64 s[78:79], exec
  s_nop 1
  s_cbranch_scc0 .re64
.ok63:
  s_xor_b32 s74, s69, 65536
  s_lshl_b32 s75, s65, 10
  s_add_u32 s74, s74, s75
  s_add_u32 m0, s74, 0
  s_add_u32 s75, s68, 33554432
  buffer_load_dwordx4 v1, s[16:19], s75 offen nt lds
  s_xor_b32 s74, s69, 65536
  s_lshl_b32 s75, s65, 10
  s_add_u32 s74, s74, s75
  s_add_u32 m0, s74, 8192
  s_add_u32 s75, s68, 33562624
  buffer_load_dwordx4 v1, s[16:19], s75 offen nt lds
  v_mov_b32_dpp v14, v12 quad_perm:[0,0,2,2] row_mask:0xf bank_mask:0xf
  v_mov_b32_dpp v15, v12 quad_perm:[1,1,3,3] row_mask:0xf bank_mask:0xf
  v_mov_b32 v20, v14
  s_nop 1
  v_max_f32_dpp v20, v20, v20 row_ror:8 row_mask:0xf bank_mask:0xf
  v_mov_b32 v21, v20
  s_nop 1
  v_permlane16_swap_b32 v20, v21
  v_max_f32 v20, v20, v21
  v_mov_b32 v21, v20
  s_nop 1
  v_permlane32_swap_b32 v20, v21
  v_max_f32 v20, v20, v21
  v_sub_f32 v21, v14, v20
  v_exp_f32 v21, v21
  s_nop 0
  v_mul_f32 v22, v21, v15
  v_mul_f32 v10, v10, v21
  v_mul_f32 v11, v11, v21
  v_add_f32_dpp v22, v22, v22 row_ror:8 row_mask:0xf bank_mask:0xf
  v_add_f32_dpp v10, v10, v10 row_ror:8 row_mask:0xf bank_mask:0xf
  v_add_f32_dpp v11, v11, v11 row_ror:8 row_mask:0xf bank_mask:0xf
  v_mov_b32 v23, v22
  s_nop 1
  v_permlane16_swap_b32 v22, v23
  v_permlane16_swap_b32 v10, v11
  v_add_f32 v22, v22, v23
  v_add_f32 v10, v10, v11
  v_mov_b32 v23, v22
  v_mov_b32 v12, v10
  s_nop 0
  v_permlane32_swap_b32 v22, v23
  v_permlane32_swap_b32 v10, v12
  v_add_f32 v22, v22, v23
  v_add_f32 v10, v10, v12
  v_rcp_f32 v22, v22
  s_nop 0
  v_mov_b32_dpp v10, v10 row_shl:4 row_mask:0xc bank_mask:0xf
  v_mul_f32 v10, v10, v22
  v_cvt_pk_bf16_f32 v18, v10, v10
  s_nop 1
  v_and_b32_dpp v10, v18, v2 row_newbcast:0 row_mask:0xf bank_mask:0xf
  v_and_b32_dpp v11, v18, v3 row_newbcast:0 row_mask:0xf bank_mask:0xf
  v_and_b32_dpp v12, v18, v4 row_newbcast:0 row_mask:0xf bank_mask:0xf
  v_and_b32_dpp v13, v18, v5 row_newbcast:0 row_mask:0xf bank_mask:0xf
  v_and_b32_dpp v14, v18, v2 row_newbcast:1 row_mask:0xf bank_mask:0xf
  v_and_b32_dpp v15, v18, v3 row_newbcast:1 row_mask:0xf bank_mask:0xf
  v_and_b32_dpp v16, v18, v4 row_newbcast:1 row_mask:0xf bank_mask:0xf
  v_and_b32_dpp v17, v18, v5 row_newbcast:1 row_mask:0xf bank_mask:0xf
  v_mfma_f32_16x16x32_bf16 v[20:23], v[24:27], v[10:13], 0
  v_and_b32_dpp v10, v18, v2 row_newbcast:2 row_mask:0xf bank_mask:0xf
  v_and_b32_dpp v11, v18, v3 row_newbcast:2 row_mask:0xf bank_mask:0xf
  v_and_b32_dpp v12, v18, v4 row_newbcast:2 row_mask:0xf bank_mask:0xf
  v_and_b32_dpp v13, v18, v5 row_newbcast:2 row_mask:0xf bank_mask:0xf
  v_mfma_f32_16x16x32_bf16 v[20:23], v[28:31], v[14:17], v[20:23]
  v_and_b32_dpp v14, v18, v2 row_newbcast:3 row_mask:0xf bank_mask:0xf
  v_and_b32_dpp v15, v18, v3 row_newbcast:3 row_mask:0xf bank_mask:0xf
  v_and_b32_dpp v16, v18, v4 row_newbcast:3 row_mask:0xf bank_mask:0xf
  v_and_b32_dpp v17, v18, v5 row_newbcast:3 row_mask:0xf bank_mask:0xf
  v_mfma_f32_16x16x32_bf16 v[20:23], v[32:35], v[10:13], v[20:23]
  v_and_b32 v9, 0xf0, v0
  v_lshlrev_b32 v9, 2, v9
  v_bfe_u32 v19, v0, 8, 2
  v_lshl_or_b32 v9, v19, 4, v9
  s_lshl_b32 s72, s65, 9
  s_add_u32 s72, s72, s70
  v_add_u32 v9, s72, v9
  v_mfma_f32_16x16x32_bf16 v[20:23], v[36:39], v[14:17], v[20:23]
  s_mov_b32 exec_lo, 0xff00ff
  s_mov_b32 exec_hi, 0xff00ff
  s_nop 5
  ds_write_b128 v9, v[20:23]
  s_mov_b64 exec, -1
)ASM"
R"ASM(
  s_xor_b32 s74, s69, 65536
  s_lshl_b32 s75, s65, 10
  s_add_u32 s74, s74, s75
  s_add_u32 m0, s74, 16384
  s_add_u32 s75, s68, 33570816
  buffer_load_dwordx4 v1, s[16:19], s75 offen nt lds
  s_add_u32 s72, s95, s97
  s_waitcnt lgkmcnt(0)
  s_barrier
  s_cmp_lg_u32 s65, 1
  s_cbranch_scc1 .ya65
  s_setprio 3
  s_mov_b32 exec_lo, 0xffffffff
  s_mov_b32 exec_hi, 0xff
  ds_read_b64 v[12:13], v6 offset:0
  ds_read_b64 v[14:15], v6 offset:512
  ds_read_b64 v[16:17], v6 offset:1024
  ds_read_b64 v[18:19], v6 offset:1536
  ds_read_b64 v[20:21], v6 offset:2048
  ds_read_b64 v[22:23], v6 offset:2560
  ds_read_b64 v[24:25], v6 offset:3072
  ds_read_b64 v[26:27], v6 offset:3584
  s_mov_b32 s78, 0xffffffff
  s_mov_b32 s79, 0xff
  s_waitcnt lgkmcnt(4)
  v_add_f32 v12, v12, v14
  v_add_f32 v13, v13, v15
  v_add_f32 v16, v16, v18
  v_add_f32 v17, v17, v19
  s_waitcnt lgkmcnt(0)
  v_add_f32 v20, v20, v22
  v_add_f32 v21, v21, v23
  v_add_f32 v24, v24, v26
  v_add_f32 v25, v25, v27
  v_add_f32 v12, v12, v16
  v_add_f32 v13, v13, v17
  v_add_f32 v20, v20, v24
  v_add_f32 v21, v21, v25
  v_add_f32 v12, v12, v20
  v_add_f32 v13, v13, v21
  v_cvt_pk_bf16_f32 v16, v12, v13
  s_mov_b64 exec, -1
  v_cndmask_b32 v16, 0, v16, s[78:79]
  ds_bpermute_b32 v12, v7, v16
  ds_bpermute_b32 v13, v7, v16 offset:4
  ds_bpermute_b32 v14, v7, v16 offset:8
  v_mov_b32 v15, s62
  s_waitcnt lgkmcnt(0)
  s_mov_b32 exec_lo, 0xffff
  s_mov_b32 exec_hi, 0x0
  buffer_store_dwordx4 v[12:15], v8, s[48:51], s72 offen sc1
  s_mov_b64 exec, -1
  s_setprio 0
.ya65:
  s_xor_b32 s70, s70, 4160
  s_lshl_b32 s72, s64, 2
  s_add_u32 s72, s72, 65
  s_sub_u32 s72, s72, 64
  s_add_u32 s62, s60, s72
  s_lshr_b32 s95, s72, 1
  s_and_b32 s95, s95, 2
  s_or_b32 s95, s95, 1
  s_mul_i32 s95, s95, 589824
  s_add_u32 s62, s62, 1
  s_or_b32 s62, s62, 0x80000000
  v_bfe_u32 v22, v0, 9, 1
  v_mul_u32_u24 v22, 393216, v22
  v_bfe_u32 v23, v0, 8, 1
  v_lshl_add_u32 v22, v23, 16, v22
  v_and_b32 v23, 0xf0, v0
  v_add_u32 v22, v22, v23
  s_lshl_b32 s73, s65, 13
  s_add_u32 s74, s73, 0
  buffer_load_dwordx4 v[28:31], v22, s[40:43], s74 offen
  s_add_u32 s74, s73, 131072
  buffer_load_dwordx4 v[32:35], v22, s[40:43], s74 offen
  s_add_u32 s74, s73, 262144
  buffer_load_dwordx4 v[36:39], v22, s[40:43], s74 offen
  s_xor_b32 s72, s70, 4160
  v_bfe_u32 v8, v0, 4, 6
  v_lshlrev_b32 v6, 3, v8
  v_add_u32 v6, s72, v6
  v_and_b32 v7, 7, v8
  v_lshlrev_b32 v7, 3, v7
  v_add_u32 v7, s72, v7
  v_lshlrev_b32 v8, 4, v8
  v_bfe_u32 v9, v0, 4, 6
  v_lshlrev_b32 v23, 4, v9
  s_lshl_b32 s76, s67, 5
  s_lshl_b32 s77, s65, 2
  s_add_u32 s76, s76, s77
  s_mul_i32 s76, s76, 256
  s_add_u32 s76, s76, s95
  s_add_u32 s76, s76, 262144
  v_bfe_u32 v9, v0, 4, 4
  v_bfe_u32 v27, v0, 9, 1
  v_mul_u32_u24 v27, 3, v27
  v_add_u32 v9, v9, v27
  v_bfe_u32 v27, v0, 8, 1
  v_lshl_or_b32 v9, v9, 1, v27
  s_add_u32 s77, s64, 2
  s_and_b32 s77, s77, 1
  s_lshl_b32 s77, s77, 10
  s_add_u32 s77, s77, 158656
  s_lshl_b32 s72, s65, 6
  s_add_u32 s77, s77, s72
  v_lshlrev_b32 v27, 2, v9
  v_add_u32 v27, s77, v27
  ds_read_b32 v22, v27
  v_bfe_u32 v25, v0, 4, 4
  v_mul_u32_u24 v25, 24, v25
  v_bfe_u32 v26, v0, 8, 1
  v_lshl_add_u32 v25, v26, 2, v25
  s_mul_i32 s72, s65, 384
  s_add_u32 s72, s72, s70
  v_add_u32 v25, s72, v25
  s_mul_i32 s72, s65, 40
  s_add_u32 s72, s72, s70
  v_lshlrev_b32 v26, 2, v9
  v_add_u32 v26, s72, v26
  s_sleep 30
  buffer_load_dwordx4 v[10:13], v23, s[48:51], s76 offen sc1
  s_waitcnt vmcnt(0)
  v_cmp_eq_u32 vcc, s62, v13
  s_nop 0
  s_mov_b64 s[78:79], vcc
  s_cmp_eq_u64 s[78:79], exec
  s_nop 1
  s_cbranch_scc1 .ok66
  s_mov_b32 s94, 0
.re67:
  s_cmp_lg_u32 s71, 0
  s_cbranch_scc1 .ok66
  s_add_u32 s94, s94, 1
  s_cmp_gt_u32 s94, 200000
  s_cselect_b32 s71, 1, 0
  buffer_load_dwordx4 v[10:13], v23, s[48:51], s76 offen sc1
  s_waitcnt vmcnt(0)
  v_cmp_eq_u32 vcc, s62, v13
  s_nop 0
  s_mov_b64 s[78:79], vcc
  s_cmp_eq_u64 s[78:79], exec
  s_nop 1
  s_cbranch_scc0 .re67
.ok66:
  s_xor_b32 s74, s69, 65536
  s_lshl_b32 s75, s65, 10
  s_add_u32 s74, s74, s75
  s_add_u32 m0, s74, 24576
  s_add_u32 s75, s68, 33579008
  buffer_load_dwordx4 v1, s[16:19], s75 offen nt lds
  s_xor_b32 s74, s69, 65536
  s_lshl_b32 s75, s65, 10
  s_add_u32 s74, s74, s75
  s_add_u32 m0, s74, 32768
  s_add_u32 s75, s68, 33587200
  buffer_load_dwordx4 v1, s[16:19], s75 offen nt lds
  v_lshlrev_b32 v14, 16, v10
  v_and_b32 v15, 0xffff0000, v10
  v_lshlrev_b32 v16, 16, v11
  v_and_b32 v17, 0xffff0000, v11
  v_lshlrev_b32 v18, 16, v12
  v_and_b32 v19, 0xffff0000, v12
  v_permlane16_swap_b32 v14, v15
  v_permlane16_swap_b32 v16, v17
  v_permlane16_swap_b32 v18, v19
  v_add_f32 v14, v14, v15
  v_add_f32 v16, v16, v17
  v_add_f32 v18, v18, v19
  v_mov_b32 v20, v14
  v_mov_b32 v21, v16
  v_mov_b32 v24, v18
  v_permlane32_swap_b32 v14, v20
  v_permlane32_swap_b32 v16, v21
  v_permlane32_swap_b32 v18, v24
  v_add_f32 v14, v14, v20
  v_add_f32 v16, v16, v21
  v_add_f32 v18, v18, v24
  s_mov_b32 exec_lo, 0x3fff3fff
  s_mov_b32 exec_hi, 0x0
  ds_write_b32 v25, v14
  ds_write_b32 v25, v16 offset:8
  ds_write_b32 v25, v18 offset:16
  s_mov_b64 exec, -1
  s_waitcnt lgkmcnt(0)
  s_barrier
  ds_read_b32 v12, v26 offset:0
  ds_read_b32 v13, v26 offset:384
  ds_read_b32 v14, v26 offset:768
  ds_read_b32 v15, v26 offset:1152
  ds_read_b32 v16, v26 offset:1536
  ds_read_b32 v17, v26 offset:1920
  ds_read_b32 v18, v26 offset:2304
  ds_read_b32 v19, v26 offset:2688
  s_waitcnt lgkmcnt(4)
  v_add_f32 v12, v12, v13
  v_add_f32 v14, v14, v15
  s_waitcnt lgkmcnt(0)
  v_add_f32 v16, v16, v17
  v_add_f32 v18, v18, v19
)ASM"
R"ASM(
  v_add_f32 v12, v12, v14
  v_add_f32 v16, v16, v18
  v_add_f32 v10, v12, v16
  v_cvt_pk_bf16_f32 v10, v10, v10
  s_nop 0
  v_and_b32 v10, 0xffff0000, v10
  v_add_f32 v12, v22, v10
  v_cvt_pk_bf16_f32 v13, v12, v12
  s_nop 0
  v_and_b32 v12, 0xffff0000, v13
  s_xor_b32 s70, s70, 4160
  v_cmp_gt_u32 vcc, 10, v9
  v_mov_b32 v27, v12
  s_nop 0
  v_cndmask_b32 v24, 0, v13, vcc
  s_nop 1
  v_and_b32_dpp v10, v24, v2 row_newbcast:0 row_mask:0xf bank_mask:0xf
  v_and_b32_dpp v11, v24, v3 row_newbcast:0 row_mask:0xf bank_mask:0xf
  v_and_b32_dpp v12, v24, v4 row_newbcast:0 row_mask:0xf bank_mask:0xf
  v_and_b32_dpp v13, v24, v5 row_newbcast:0 row_mask:0xf bank_mask:0xf
  v_and_b32_dpp v14, v24, v2 row_newbcast:1 row_mask:0xf bank_mask:0xf
  v_and_b32_dpp v15, v24, v3 row_newbcast:1 row_mask:0xf bank_mask:0xf
  v_and_b32_dpp v16, v24, v4 row_newbcast:1 row_mask:0xf bank_mask:0xf
  v_and_b32_dpp v17, v24, v5 row_newbcast:1 row_mask:0xf bank_mask:0xf
  v_mfma_f32_16x16x32_bf16 v[20:23], v[28:31], v[10:13], 0
  v_and_b32_dpp v10, v24, v2 row_newbcast:2 row_mask:0xf bank_mask:0xf
  v_and_b32_dpp v11, v24, v3 row_newbcast:2 row_mask:0xf bank_mask:0xf
  v_and_b32_dpp v12, v24, v4 row_newbcast:2 row_mask:0xf bank_mask:0xf
  v_and_b32_dpp v13, v24, v5 row_newbcast:2 row_mask:0xf bank_mask:0xf
  v_mfma_f32_16x16x32_bf16 v[20:23], v[32:35], v[14:17], v[20:23]
  s_add_u32 s77, s64, 2
  s_and_b32 s77, s77, 1
  s_lshl_b32 s77, s77, 10
  s_add_u32 s77, s77, 158144
  s_lshl_b32 s72, s65, 6
  s_add_u32 s77, s77, s72
  v_lshlrev_b32 v9, 2, v9
  v_add_u32 v9, s77, v9
  v_mfma_f32_16x16x32_bf16 v[20:23], v[36:39], v[10:13], v[20:23]
  s_mov_b32 exec_lo, 0x70007
  s_mov_b32 exec_hi, 0x30003
  ds_write_b32 v9, v27
  s_mov_b64 exec, -1
  v_and_b32 v9, 0xf0, v0
  v_lshlrev_b32 v9, 2, v9
  v_bfe_u32 v25, v0, 8, 2
  v_lshl_or_b32 v9, v25, 4, v9
  s_lshl_b32 s72, s65, 9
  s_add_u32 s72, s72, s70
  v_add_u32 v9, s72, v9
  s_mov_b32 s78, 0x70007
  s_mov_b32 s79, 0x30003
  v_cndmask_b32 v16, 0, v27, s[78:79]
  v_mul_f32 v17, v16, v16
  s_nop 0
  v_add_f32_dpp v16, v16, v16 quad_perm:[1,0,3,2] row_mask:0xf bank_mask:0xf
  v_add_f32_dpp v17, v17, v17 quad_perm:[1,0,3,2] row_mask:0xf bank_mask:0xf
  s_nop 0
  v_add_f32_dpp v16, v16, v16 quad_perm:[2,3,0,1] row_mask:0xf bank_mask:0xf
  v_add_f32_dpp v17, v17, v17 quad_perm:[2,3,0,1] row_mask:0xf bank_mask:0xf
  s_nop 1
  v_permlane16_swap_b32 v16, v17
  v_add_f32 v16, v16, v17
  v_mov_b32 v18, v16
  s_nop 1
  v_permlane32_swap_b32 v16, v18
  v_add_f32 v16, v16, v18
  s_lshl_b32 s72, s65, 3
  s_add_u32 s72, s72, s70
  v_bfe_u32 v19, v0, 8, 1
  v_lshlrev_b32 v19, 2, v19
  v_add_u32 v19, s72, v19
  s_mov_b32 exec_lo, 0x10001
  s_mov_b32 exec_hi, 0x0
  ds_write_b32 v19, v16 offset:4096
  s_mov_b64 exec, -1
  s_mov_b32 exec_lo, 0xff00ff
  s_mov_b32 exec_hi, 0xff00ff
  ds_write_b128 v9, v[20:23]
  s_mov_b64 exec, -1
  s_xor_b32 s74, s69, 65536
  s_lshl_b32 s75, s65, 10
  s_add_u32 s74, s74, s75
  s_add_u32 m0, s74, 40960
  s_add_u32 s75, s68, 33595392
  buffer_load_dwordx4 v1, s[16:19], s75 offen nt lds
  s_waitcnt lgkmcnt(0)
  s_barrier
  s_cmp_lg_u32 s65, 2
  s_cbranch_scc1 .ub68
  s_setprio 3
  ds_read_b64 v[12:13], v6 offset:0
  ds_read_b64 v[14:15], v6 offset:512
  ds_read_b64 v[16:17], v6 offset:1024
  ds_read_b64 v[18:19], v6 offset:1536
  ds_read_b64 v[20:21], v6 offset:2048
  ds_read_b64 v[22:23], v6 offset:2560
  ds_read_b64 v[24:25], v6 offset:3072
  ds_read_b64 v[26:27], v6 offset:3584
  ds_read_b64 v[28:29], v7 offset:4096
  s_add_u32 s72, s95, s98
  s_waitcnt lgkmcnt(5)
  v_add_f32 v12, v12, v14
  v_add_f32 v13, v13, v15
  v_add_f32 v16, v16, v18
  v_add_f32 v17, v17, v19
  s_waitcnt lgkmcnt(1)
  v_add_f32 v20, v20, v22
  v_add_f32 v21, v21, v23
  v_add_f32 v24, v24, v26
  v_add_f32 v25, v25, v27
  v_add_f32 v12, v12, v16
  v_add_f32 v13, v13, v17
  v_add_f32 v20, v20, v24
  v_add_f32 v21, v21, v25
  v_add_f32 v12, v12, v20
  v_add_f32 v13, v13, v21
  s_waitcnt lgkmcnt(0)
  v_add_f32_dpp v28, v28, v28 quad_perm:[1,0,3,2] row_mask:0xf bank_mask:0xf
  v_add_f32_dpp v29, v29, v29 quad_perm:[1,0,3,2] row_mask:0xf bank_mask:0xf
  s_nop 0
  v_add_f32_dpp v28, v28, v28 quad_perm:[2,3,0,1] row_mask:0xf bank_mask:0xf
  v_add_f32_dpp v29, v29, v29 quad_perm:[2,3,0,1] row_mask:0xf bank_mask:0xf
  s_nop 0
  v_add_f32_dpp v28, v28, v28 row_half_mirror row_mask:0xf bank_mask:0xf
  v_add_f32_dpp v29, v29, v29 row_half_mirror row_mask:0xf bank_mask:0xf
  v_cndmask_b32 v14, v28, v29, s[80:81]
  v_mov_b32 v15, s62
  buffer_store_dwordx4 v[12:15], v8, s[48:51], s72 offen sc1
  s_setprio 0
  s_nop 0
.ub68:
  s_xor_b32 s70, s70, 4160
  s_lshl_b32 s72, s64, 2
  s_add_u32 s72, s72, 65
  s_sub_u32 s72, s72, 64
  s_add_u32 s62, s60, s72
  s_lshr_b32 s95, s72, 1
  s_and_b32 s95, s95, 2
  s_or_b32 s95, s95, 1
  s_mul_i32 s95, s95, 589824
  s_add_u32 s62, s62, 1
  s_or_b32 s62, s62, 0x80000000
  v_bfe_u32 v9, v0, 4, 6
  v_lshrrev_b32 v20, 3, v9
  v_mul_u32_u24 v20, 1024, v20
  v_and_b32 v9, 7, v9
  v_lshl_add_u32 v20, v9, 4, v20
  s_mul_i32 s76, s66, 8192
  s_add_u32 s76, s76, s95
  s_add_u32 s76, s76, 327680
  s_lshl_b32 s73, s65, 7
  s_add_u32 s76, s76, s73
  v_bfe_u32 v18, v0, 8, 1
  v_lshl_or_b32 v19, v9, 1, v18
  v_lshlrev_b32 v19, 2, v19
  s_lshl_b32 s73, s65, 6
  s_add_u32 s73, s73, 157120
  v_add_u32 v19, s73, v19
  ds_read_b32 v22, v19
  v_lshl_or_b32 v18, v9, 1, v18
  v_lshlrev_b32 v18, 4, v18
  s_lshl_b32 s73, s65, 1
  s_add_u32 s73, s73, 155840
  v_add_u32 v18, s73, v18
  s_sleep 20
  buffer_load_dwordx4 v[10:13], v20, s[48:51], s76 offen sc1
  s_waitcnt vmcnt(0)
  v_cmp_eq_u32 vcc, s62, v13
  s_nop 0
  s_mov_b64 s[78:79], vcc
  s_cmp_eq_u64 s[78:79], exec
  s_nop 1
  s_cbranch_scc1 .ok69
  s_mov_b32 s94, 0
.re70:
  s_cmp_lg_u32 s71, 0
  s_cbranch_scc1 .ok69
  s_add_u32 s94, s94, 1
  s_cmp_gt_u32 s94, 200000
  s_cselect_b32 s71, 1, 0
  buffer_load_dwordx4 v[10:13], v20, s[48:51], s76 offen sc1
  s_waitcnt vmcnt(0)
  v_cmp_eq_u32 vcc, s62, v13
  s_nop 0
  s_mov_b64 s[78:79], vcc
  s_cmp_eq_u64 s[78:79], exec
  s_nop 1
  s_cbranch_scc0 .re70
.ok69:
  s_xor_b32 s74, s69, 65536
  s_lshl_b32 s75, s65, 10
  s_add_u32 s74, s74, s75
  s_add_u32 m0, s74, 49152
  s_add_u32 s75, s68, 33603584
  buffer_load_dwordx4 v1, s[16:19], s75 offen nt lds
  v_add_f32_dpp v12, v12, v12 row_ror:8 row_mask:0xf bank_mask:0xf
  v_add_f32_dpp v10, v10, v10 row_ror:8 row_mask:0xf bank_mask:0xf
  v_add_f32_dpp v11, v11, v11 row_ror:8 row_mask:0xf bank_mask:0xf
  v_mov_b32 v16, v12
)ASM"
R"ASM(
  s_nop 1
  v_permlane16_swap_b32 v12, v16
  v_permlane16_swap_b32 v10, v11
  v_add_f32 v12, v12, v16
  v_add_f32 v10, v10, v11
  v_mov_b32 v16, v12
  v_mov_b32 v17, v10
  s_nop 0
  v_permlane32_swap_b32 v12, v16
  v_permlane32_swap_b32 v10, v17
  v_add_f32 v12, v12, v16
  v_add_f32 v10, v10, v17
  s_nop 0
  v_mov_b32_dpp v14, v12 quad_perm:[0,0,2,2] row_mask:0xf bank_mask:0xf
  v_mov_b32_dpp v15, v12 quad_perm:[1,1,3,3] row_mask:0xf bank_mask:0xf
  v_mov_b32 v16, 0x3727c5ac
  v_mul_f32 v14, 0x3acccccd, v14
  v_mul_f32 v15, 0x3acccccd, v15
  v_fma_f32 v15, -v14, v14, v15
  v_max_f32 v15, 0, v15
  v_add_f32 v15, v15, v16
  v_rsq_f32 v15, v15
  s_waitcnt lgkmcnt(0)
  v_fma_f32 v10, -v14, v22, v10
  v_mul_f32 v10, v10, v15
  v_cvt_pk_bf16_f32 v10, v10, v10
  s_mov_b32 exec_lo, 0xff00ff
  s_mov_b32 exec_hi, 0x0
  ds_write_b16 v18, v10
  s_mov_b64 exec, -1
  s_xor_b32 s74, s69, 65536
  s_lshl_b32 s75, s65, 10
  s_add_u32 s74, s74, s75
  s_add_u32 m0, s74, 57344
  s_add_u32 s75, s68, 33611776
  buffer_load_dwordx4 v1, s[16:19], s75 offen nt lds
  s_waitcnt lgkmcnt(0)
  s_barrier
  s_mov_b32 s76, 155840
  v_and_b32 v9, 0xff, v1
  v_add_u32 v9, s76, v9
  v_mov_b32 v22, 155776
  v_cndmask_b32 v23, v22, v9, s[84:85]
  ds_read_b128 v[24:27], v23
  s_nop 0
  v_cndmask_b32 v23, v22, v9, s[86:87]
  ds_read_b128 v[28:31], v23
  s_nop 0
  v_cndmask_b32 v23, v22, v9, s[88:89]
  ds_read_b128 v[32:35], v23
  s_nop 0
  v_cndmask_b32 v23, v22, v9, s[90:91]
  ds_read_b128 v[36:39], v23
  v_add_u32 v9, s69, v0
  ds_read_b128 v[14:17], v9 offset:0
  ds_read_b128 v[18:21], v9 offset:8192
  s_waitcnt lgkmcnt(2)
  v_mfma_f32_16x16x32_bf16 v[10:13], v[40:43], v[24:27], 0
  v_mfma_f32_16x16x32_bf16 v[10:13], v[44:47], v[28:31], v[10:13]
  v_mfma_f32_16x16x32_bf16 v[10:13], v[48:51], v[32:35], v[10:13]
  v_mfma_f32_16x16x32_bf16 v[10:13], v[52:55], v[36:39], v[10:13]
  s_nop 7
  v_cndmask_b32 v22, v10, v11, s[80:81]
  v_cndmask_b32 v23, v12, v13, s[80:81]
  s_waitcnt lgkmcnt(1)
  v_mfma_f32_16x16x32_bf16 v[10:13], v[14:17], v[24:27], 0
  ds_read_b128 v[14:17], v9 offset:16384
  s_waitcnt lgkmcnt(1)
  v_mfma_f32_16x16x32_bf16 v[10:13], v[18:21], v[28:31], v[10:13]
  ds_read_b128 v[18:21], v9 offset:24576
  s_waitcnt lgkmcnt(1)
  v_mfma_f32_16x16x32_bf16 v[10:13], v[14:17], v[32:35], v[10:13]
  ds_read_b128 v[14:17], v9 offset:32768
  s_waitcnt lgkmcnt(1)
  v_mfma_f32_16x16x32_bf16 v[10:13], v[18:21], v[36:39], v[10:13]
  ds_read_b128 v[18:21], v9 offset:40960
  s_nop 6
  v_cndmask_b32 v10, v10, v11, s[80:81]
  v_cndmask_b32 v12, v12, v13, s[80:81]
  v_cndmask_b32 v11, v10, v23, s[82:83]
  v_cndmask_b32 v13, v22, v12, s[82:83]
  s_nop 1
  v_add_f32_dpp v23, v13, v11 quad_perm:[2,3,0,1] row_mask:0xf bank_mask:0xf
  s_waitcnt lgkmcnt(1)
  v_mfma_f32_16x16x32_bf16 v[10:13], v[14:17], v[24:27], 0
  ds_read_b128 v[14:17], v9 offset:49152
  s_waitcnt lgkmcnt(1)
  v_mfma_f32_16x16x32_bf16 v[10:13], v[18:21], v[28:31], v[10:13]
  ds_read_b128 v[18:21], v9 offset:57344
  s_waitcnt lgkmcnt(1)
  v_mfma_f32_16x16x32_bf16 v[10:13], v[14:17], v[32:35], v[10:13]
  s_waitcnt lgkmcnt(0)
  v_mfma_f32_16x16x32_bf16 v[10:13], v[18:21], v[36:39], v[10:13]
  s_nop 7
  v_cndmask_b32 v22, v10, v11, s[80:81]
  v_cndmask_b32 v9, v12, v13, s[80:81]
  v_mfma_f32_16x16x32_bf16 v[10:13], v[56:59], v[24:27], 0
  v_mfma_f32_16x16x32_bf16 v[10:13], v[60:63], v[28:31], v[10:13]
  v_mfma_f32_16x16x32_bf16 v[10:13], v[64:67], v[32:35], v[10:13]
  v_mfma_f32_16x16x32_bf16 v[10:13], v[68:71], v[36:39], v[10:13]
  s_nop 7
  v_cndmask_b32 v10, v10, v11, s[80:81]
  v_cndmask_b32 v12, v12, v13, s[80:81]
  v_cndmask_b32 v11, v22, v12, s[82:83]
  v_cndmask_b32 v13, v10, v9, s[82:83]
  s_nop 1
  v_add_f32_dpp v22, v13, v11 quad_perm:[2,3,0,1] row_mask:0xf bank_mask:0xf
  v_cndmask_b32 v10, v23, v22, s[80:81]
  v_cndmask_b32 v11, v22, v23, s[80:81]
  s_nop 1
  v_add_f32_dpp v12, v11, v10 quad_perm:[1,0,3,2] row_mask:0xf bank_mask:0xf
  s_nop 1
  v_max_f32_dpp v13, v12, v12 row_ror:8 row_mask:0xf bank_mask:0xf
  s_nop 1
  v_max_f32_dpp v13, v13, v13 row_ror:4 row_mask:0xf bank_mask:0xf
  s_nop 1
  v_max_f32_dpp v13, v13, v13 quad_perm:[2,3,0,1] row_mask:0xf bank_mask:0xf
  s_nop 1
  v_max_f32_dpp v13, v13, v13 quad_perm:[1,0,3,2] row_mask:0xf bank_mask:0xf
  v_mov_b32 v14, v13
  s_nop 1
  v_permlane16_swap_b32 v13, v14
  v_max_f32 v13, v13, v14
  v_mov_b32 v14, v13
  s_nop 1
  v_permlane32_swap_b32 v13, v14
  v_max_f32 v13, v13, v14
  v_mul_f32 v37, s92, v13
  v_fma_f32 v14, v12, s92, -v37
  v_exp_f32 v38, v14
  s_nop 0
  v_cvt_pk_bf16_f32 v36, v38, v38
  s_nop 1
  v_and_b32_dpp v10, v36, v2 row_newbcast:0 row_mask:0xf bank_mask:0xf
  v_and_b32_dpp v11, v36, v3 row_newbcast:0 row_mask:0xf bank_mask:0xf
  v_and_b32_dpp v12, v36, v4 row_newbcast:0 row_mask:0xf bank_mask:0xf
  v_and_b32_dpp v13, v36, v5 row_newbcast:0 row_mask:0xf bank_mask:0xf
  v_and_b32_dpp v14, v36, v2 row_newbcast:4 row_mask:0xf bank_mask:0xf
  v_and_b32_dpp v15, v36, v3 row_newbcast:4 row_mask:0xf bank_mask:0xf
  v_and_b32_dpp v16, v36, v4 row_newbcast:4 row_mask:0xf bank_mask:0xf
  v_and_b32_dpp v17, v36, v5 row_newbcast:4 row_mask:0xf bank_mask:0xf
  v_mfma_f32_16x16x32_bf16 v[18:21], v[72:75], v[10:13], 0
  s_add_u32 s75, s68, 33619968
  buffer_load_dwordx4 v[40:43], v1, s[16:19], s75 offen nt
  v_and_b32_dpp v10, v36, v2 row_newbcast:8 row_mask:0xf bank_mask:0xf
  v_and_b32_dpp v11, v36, v3 row_newbcast:8 row_mask:0xf bank_mask:0xf
  v_and_b32_dpp v12, v36, v4 row_newbcast:8 row_mask:0xf bank_mask:0xf
  v_and_b32_dpp v13, v36, v5 row_newbcast:8 row_mask:0xf bank_mask:0xf
  v_mfma_f32_16x16x32_bf16 v[18:21], v[76:79], v[14:17], v[18:21]
  v_and_b32_dpp v14, v36, v2 row_newbcast:12 row_mask:0xf bank_mask:0xf
  v_and_b32_dpp v15, v36, v3 row_newbcast:12 row_mask:0xf bank_mask:0xf
  v_and_b32_dpp v16, v36, v4 row_newbcast:12 row_mask:0xf bank_mask:0xf
  v_and_b32_dpp v17, v36, v5 row_newbcast:12 row_mask:0xf bank_mask:0xf
  v_mfma_f32_16x16x32_bf16 v[18:21], v[80:83], v[10:13], v[18:21]
  v_and_b32_dpp v10, v36, v2 row_newbcast:1 row_mask:0xf bank_mask:0xf
  v_and_b32_dpp v11, v36, v3 row_newbcast:1 row_mask:0xf bank_mask:0xf
  v_and_b32_dpp v12, v36, v4 row_newbcast:1 row_mask:0xf bank_mask:0xf
  v_and_b32_dpp v13, v36, v5 row_newbcast:1 row_mask:0xf bank_mask:0xf
  v_mfma_f32_16x16x32_bf16 v[18:21], v[84:87], v[14:17], v[18:21]
  s_add_u32 s75, s68, 33628160
  buffer_load_dwordx4 v[44:47], v1, s[16:19], s75 offen nt
  v_and_b32_dpp v14, v36, v2 row_newbcast:5 row_mask:0xf bank_mask:0xf
  v_and_b32_dpp v15, v36, v3 row_newbcast:5 row_mask:0xf bank_mask:0xf
  v_and_b32_dpp v16, v36, v4 row_newbcast:5 row_mask:0xf bank_mask:0xf
  v_and_b32_dpp v17, v36, v5 row_newbcast:5 row_mask:0xf bank_mask:0xf
  v_mfma_f32_16x16x32_bf16 v[18:21], v[88:91], v[10:13], v[18:21]
  v_and_b32_dpp v10, v36, v2 row_newbcast:9 row_mask:0xf bank_mask:0xf
  v_and_b32_dpp v11, v36, v3 row_newbcast:9 row_mask:0xf bank_mask:0xf
  v_and_b32_dpp v12, v36, v4 row_newbcast:9 row_mask:0xf bank_mask:0xf
  v_and_b32_dpp v13, v36, v5 row_newbcast:9 row_mask:0xf bank_mask:0xf
  v_mfma_f32_16x16x32_bf16 v[18:21], v[92:95], v[14:17], v[18:21]
  v_and_b32_dpp v14, v36, v2 row_newbcast:13 row_mask:0xf bank_mask:0xf
  v_and_b32_dpp v15, v36, v3 row_newbcast:13 row_mask:0xf bank_mask:0xf
  v_and_b32_dpp v16, v36, v4 row_newbcast:13 row_mask:0xf bank_mask:0xf
  v_and_b32_dpp v17, v36, v5 row_newbcast:13 row_mask:0xf bank_mask:0xf
  v_mfma_f32_16x16x32_bf16 v[18:21], v[96:99], v[10:13], v[18:21]
  s_add_u32 s75, s68, 33636352
  buffer_load_dwordx4 v[48:51], v1, s[16:19], s75 offen nt
  v_and_b32_dpp v10, v36, v2 row_newbcast:2 row_mask:0xf bank_mask:0xf
  v_and_b32_dpp v11, v36, v3 row_newbcast:2 row_mask:0xf bank_mask:0xf
  v_and_b32_dpp v12, v36, v4 row_newbcast:2 row_mask:0xf bank_mask:0xf
  v_and_b32_dpp v13, v36, v5 row_newbcast:2 row_mask:0xf bank_mask:0xf
  v_mfma_f32_16x16x32_bf16 v[18:21], v[100:103], v[14:17], v[18:21]
  v_and_b32_dpp v14, v36, v2 row_newbcast:6 row_mask:0xf bank_mask:0xf
  v_and_b32_dpp v15, v36, v3 row_newbcast:6 row_mask:0xf bank_mask:0xf
  v_and_b32_dpp v16, v36, v4 row_newbcast:6 row_mask:0xf bank_mask:0xf
  v_and_b32_dpp v17, v36, v5 row_newbcast:6 row_mask:0xf bank_mask:0xf
  v_mfma_f32_16x16x32_bf16 v[18:21], v[104:107], v[10:13], v[18:21]
  v_and_b32_dpp v10, v36, v2 row_newbcast:10 row_mask:0xf bank_mask:0xf
  v_and_b32_dpp v11, v36, v3 row_newbcast:10 row_mask:0xf bank_mask:0xf
  v_and_b32_dpp v12, v36, v4 row_newbcast:10 row_mask:0xf bank_mask:0xf
  v_and_b32_dpp v13, v36, v5 row_newbcast:10 row_mask:0xf bank_mask:0xf
  v_mfma_f32_16x16x32_bf16 v[18:21], v[108:111], v[14:17], v[18:21]
  s_add_u32 s75, s68, 33644544
  buffer_load_dwordx4 v[52:55], v1, s[16:19], s75 offen nt
  v_and_b32_dpp v14, v36, v2 row_newbcast:14 row_mask:0xf bank_mask:0xf
  v_and_b32_dpp v15, v36, v3 row_newbcast:14 row_mask:0xf bank_mask:0xf
  v_and_b32_dpp v16, v36, v4 row_newbcast:14 row_mask:0xf bank_mask:0xf
  v_and_b32_dpp v17, v36, v5 row_newbcast:14 row_mask:0xf bank_mask:0xf
  v_mfma_f32_16x16x32_bf16 v[18:21], v[112:115], v[10:13], v[18:21]
)ASM"
R"ASM(
  v_and_b32_dpp v10, v36, v2 row_newbcast:3 row_mask:0xf bank_mask:0xf
  v_and_b32_dpp v11, v36, v3 row_newbcast:3 row_mask:0xf bank_mask:0xf
  v_and_b32_dpp v12, v36, v4 row_newbcast:3 row_mask:0xf bank_mask:0xf
  v_and_b32_dpp v13, v36, v5 row_newbcast:3 row_mask:0xf bank_mask:0xf
  v_mfma_f32_16x16x32_bf16 v[18:21], v[116:119], v[14:17], v[18:21]
  v_and_b32_dpp v14, v36, v2 row_newbcast:7 row_mask:0xf bank_mask:0xf
  v_and_b32_dpp v15, v36, v3 row_newbcast:7 row_mask:0xf bank_mask:0xf
  v_and_b32_dpp v16, v36, v4 row_newbcast:7 row_mask:0xf bank_mask:0xf
  v_and_b32_dpp v17, v36, v5 row_newbcast:7 row_mask:0xf bank_mask:0xf
  v_mfma_f32_16x16x32_bf16 v[18:21], v[120:123], v[10:13], v[18:21]
  v_and_b32_dpp v10, v36, v2 row_newbcast:11 row_mask:0xf bank_mask:0xf
  v_and_b32_dpp v11, v36, v3 row_newbcast:11 row_mask:0xf bank_mask:0xf
  v_and_b32_dpp v12, v36, v4 row_newbcast:11 row_mask:0xf bank_mask:0xf
  v_and_b32_dpp v13, v36, v5 row_newbcast:11 row_mask:0xf bank_mask:0xf
  v_mfma_f32_16x16x32_bf16 v[18:21], v[124:127], v[14:17], v[18:21]
  v_and_b32_dpp v14, v36, v2 row_newbcast:15 row_mask:0xf bank_mask:0xf
  v_and_b32_dpp v15, v36, v3 row_newbcast:15 row_mask:0xf bank_mask:0xf
  v_and_b32_dpp v16, v36, v4 row_newbcast:15 row_mask:0xf bank_mask:0xf
  v_and_b32_dpp v17, v36, v5 row_newbcast:15 row_mask:0xf bank_mask:0xf
  v_mfma_f32_16x16x32_bf16 v[18:21], v[128:131], v[10:13], v[18:21]
  s_nop 0
  v_mfma_f32_16x16x32_bf16 v[18:21], v[132:135], v[14:17], v[18:21]
  v_add_f32_dpp v22, v38, v38 row_ror:8 row_mask:0xf bank_mask:0xf
  s_nop 1
  v_add_f32_dpp v22, v22, v22 row_ror:4 row_mask:0xf bank_mask:0xf
  s_nop 1
  v_add_f32_dpp v22, v22, v22 quad_perm:[2,3,0,1] row_mask:0xf bank_mask:0xf
  s_nop 1
  v_add_f32_dpp v22, v22, v22 quad_perm:[1,0,3,2] row_mask:0xf bank_mask:0xf
  v_mov_b32 v23, v22
  s_nop 1
  v_permlane16_swap_b32 v22, v23
  v_add_f32 v22, v22, v23
  v_mov_b32 v23, v22
  s_nop 1
  v_permlane32_swap_b32 v22, v23
  v_add_f32 v22, v22, v23
  v_and_b32 v9, 0xf0, v0
  v_lshlrev_b32 v9, 2, v9
  v_bfe_u32 v10, v0, 8, 2
  v_lshl_or_b32 v9, v10, 4, v9
  s_lshl_b32 s72, s65, 9
  s_add_u32 s72, s72, s70
  v_add_u32 v9, s72, v9
  s_mov_b32 exec_lo, 0xff00ff
  s_mov_b32 exec_hi, 0xff00ff
  ds_write_b128 v9, v[18:21]
  s_mov_b32 exec_lo, 0x1
  s_mov_b32 exec_hi, 0x0
  s_lshl_b32 s72, s65, 3
  s_add_u32 s72, s72, s70
  v_mov_b32 v9, s72
  v_mov_b32 v23, v22
  v_mov_b32 v22, v37
  ds_write_b64 v9, v[22:23] offset:4096
  s_mov_b64 exec, -1
  v_bfe_u32 v9, v0, 4, 6
  v_lshlrev_b32 v28, 3, v9
  v_add_u32 v28, s70, v28
  v_and_b32 v29, 7, v9
  v_lshlrev_b32 v29, 3, v29
  v_add_u32 v29, s70, v29
  v_lshlrev_b32 v9, 4, v9
  s_lshl_b32 s72, s64, 2
  s_add_u32 s72, s72, 66
  s_sub_u32 s72, s72, 64
  s_add_u32 s62, s60, s72
  s_lshr_b32 s95, s72, 1
  s_and_b32 s95, s95, 2
  s_or_b32 s95, s95, 0
  s_mul_i32 s95, s95, 589824
  s_add_u32 s62, s62, 1
  s_or_b32 s62, s62, 0x80000000
  s_add_u32 s72, s95, s96
  s_waitcnt lgkmcnt(0)
  s_barrier
  s_cmp_lg_u32 s65, 0
  s_cbranch_scc1 .mg71
  s_setprio 3
  ds_read_b64 v[26:27], v29 offset:4096
  ds_read_b64 v[10:11], v28 offset:0
  ds_read_b64 v[12:13], v28 offset:512
  ds_read_b64 v[14:15], v28 offset:1024
  ds_read_b64 v[16:17], v28 offset:1536
  ds_read_b64 v[18:19], v28 offset:2048
  ds_read_b64 v[20:21], v28 offset:2560
  ds_read_b64 v[22:23], v28 offset:3072
  ds_read_b64 v[24:25], v28 offset:3584
  s_waitcnt lgkmcnt(8)
  v_mov_b32 v30, v26
  s_nop 1
  v_max_f32_dpp v30, v30, v30 quad_perm:[1,0,3,2] row_mask:0xf bank_mask:0xf
  s_nop 1
  v_max_f32_dpp v30, v30, v30 quad_perm:[2,3,0,1] row_mask:0xf bank_mask:0xf
  s_nop 1
  v_max_f32_dpp v30, v30, v30 row_half_mirror row_mask:0xf bank_mask:0xf
  v_sub_f32 v31, v26, v30
  v_exp_f32 v31, v31
  s_nop 0
  v_mul_f32 v32, v31, v27
  v_mov_b32 v36, 0
  v_mov_b32 v37, 0
  v_mov_b32 v39, s62
  v_add_f32_dpp v32, v32, v32 quad_perm:[1,0,3,2] row_mask:0xf bank_mask:0xf
  s_nop 1
  v_add_f32_dpp v32, v32, v32 quad_perm:[2,3,0,1] row_mask:0xf bank_mask:0xf
  s_nop 1
  v_add_f32_dpp v32, v32, v32 row_half_mirror row_mask:0xf bank_mask:0xf
  s_waitcnt lgkmcnt(0)
  v_fmac_f32_dpp v36, v31, v10 row_newbcast:0 row_mask:0xf bank_mask:0xf
  v_fmac_f32_dpp v37, v31, v11 row_newbcast:0 row_mask:0xf bank_mask:0xf
  s_nop 0
  v_fmac_f32_dpp v36, v31, v12 row_newbcast:1 row_mask:0xf bank_mask:0xf
  v_fmac_f32_dpp v37, v31, v13 row_newbcast:1 row_mask:0xf bank_mask:0xf
  s_nop 0
  v_fmac_f32_dpp v36, v31, v14 row_newbcast:2 row_mask:0xf bank_mask:0xf
  v_fmac_f32_dpp v37, v31, v15 row_newbcast:2 row_mask:0xf bank_mask:0xf
  s_nop 0
  v_fmac_f32_dpp v36, v31, v16 row_newbcast:3 row_mask:0xf bank_mask:0xf
  v_fmac_f32_dpp v37, v31, v17 row_newbcast:3 row_mask:0xf bank_mask:0xf
  s_nop 0
  v_fmac_f32_dpp v36, v31, v18 row_newbcast:4 row_mask:0xf bank_mask:0xf
  v_fmac_f32_dpp v37, v31, v19 row_newbcast:4 row_mask:0xf bank_mask:0xf
  s_nop 0
  v_fmac_f32_dpp v36, v31, v20 row_newbcast:5 row_mask:0xf bank_mask:0xf
  v_fmac_f32_dpp v37, v31, v21 row_newbcast:5 row_mask:0xf bank_mask:0xf
  s_nop 0
  v_fmac_f32_dpp v36, v31, v22 row_newbcast:6 row_mask:0xf bank_mask:0xf
  v_fmac_f32_dpp v37, v31, v23 row_newbcast:6 row_mask:0xf bank_mask:0xf
  s_nop 0
  v_fmac_f32_dpp v36, v31, v24 row_newbcast:7 row_mask:0xf bank_mask:0xf
  v_fmac_f32_dpp v37, v31, v25 row_newbcast:7 row_mask:0xf bank_mask:0xf
  v_cndmask_b32 v38, v30, v32, s[80:81]
  buffer_store_dwordx4 v[36:39], v9, s[48:51], s72 offen sc1
  s_setprio 0
  s_nop 0
.mg71:
  s_xor_b32 s70, s70, 4160
  s_add_u32 s64, s64, 1
  s_add_u32 s68, s68, 33554432
  s_xor_b32 s69, s69, 65536
  s_cmp_lt_u32 s64, 4
  s_cbranch_scc1 .Lbatch
  s_lshl_b32 s72, s64, 2
  s_add_u32 s72, s72, 62
  s_sub_u32 s72, s72, 64
  s_add_u32 s62, s60, s72
  s_lshr_b32 s95, s72, 1
  s_and_b32 s95, s95, 2
  s_or_b32 s95, s95, 0
  s_mul_i32 s95, s95, 589824
  s_add_u32 s62, s62, 1
  s_or_b32 s62, s62, 0x80000000
  v_bfe_u32 v19, v0, 8, 1
  v_mul_u32_u24 v19, 10240, v19
  v_bfe_u32 v18, v0, 9, 1
  v_mul_u32_u24 v18, 81920, v18
  v_add_u32 v19, v19, v18
  v_and_b32 v18, 0xf0, v0
  v_add_u32 v19, v19, v18
  s_mul_i32 s73, s65, 1280
  s_mov_b32 exec_lo, 0x3ff03ff
  s_mov_b32 exec_hi, 0x3ff03ff
  s_add_u32 s74, s73, 0
  buffer_load_dwordx4 v[24:27], v19, s[32:35], s74 offen
  s_add_u32 s74, s73, 20480
  buffer_load_dwordx4 v[28:31], v19, s[32:35], s74 offen
  s_add_u32 s74, s73, 40960
  buffer_load_dwordx4 v[32:35], v19, s[32:35], s74 offen
  s_add_u32 s74, s73, 61440
  buffer_load_dwordx4 v[36:39], v19, s[32:35], s74 offen
  s_mov_b64 exec, -1
  v_bfe_u32 v8, v0, 4, 6
  v_mul_u32_u24 v6, 52, v8
  v_lshrrev_b32 v6, 8, v6
  v_mul_u32_u24 v7, 5, v6
  v_sub_u32 v7, v8, v7
  v_lshlrev_b32 v6, 6, v6
  v_lshl_add_u32 v6, v7, 3, v6
  v_add_u32 v6, s70, v6
  v_mul_u32_u24 v7, 12, v8
  v_lshlrev_b32 v8, 4, v8
  v_bfe_u32 v9, v0, 4, 6
  v_lshrrev_b32 v20, 3, v9
  v_mul_u32_u24 v20, 1024, v20
  v_and_b32 v9, 7, v9
  v_lshl_add_u32 v20, v9, 4, v20
  s_mul_i32 s76, s66, 8192
  s_add_u32 s76, s76, s95
  s_lshl_b32 s73, s65, 7
  s_add_u32 s76, s76, s73
  s_sleep 8
  buffer_load_dwordx4 v[10:13], v20, s[48:51], s76 offen sc1
  s_waitcnt vmcnt(0)
  v_cmp_eq_u32 vcc, s62, v13
  s_nop 0
  s_mov_b64 s[78:79], vcc
  s_cmp_eq_u64 s[78:79], exec
  s_nop 1
  s_cbranch_scc1 .ok72
)ASM"
R"ASM(
  s_mov_b32 s94, 0
.re73:
  s_cmp_lg_u32 s71, 0
  s_cbranch_scc1 .ok72
  s_add_u32 s94, s94, 1
  s_cmp_gt_u32 s94, 200000
  s_cselect_b32 s71, 1, 0
  buffer_load_dwordx4 v[10:13], v20, s[48:51], s76 offen sc1
  s_waitcnt vmcnt(0)
  v_cmp_eq_u32 vcc, s62, v13
  s_nop 0
  s_mov_b64 s[78:79], vcc
  s_cmp_eq_u64 s[78:79], exec
  s_nop 1
  s_cbranch_scc0 .re73
.ok72:
  v_mov_b32_dpp v14, v12 quad_perm:[0,0,2,2] row_mask:0xf bank_mask:0xf
  v_mov_b32_dpp v15, v12 quad_perm:[1,1,3,3] row_mask:0xf bank_mask:0xf
  v_mov_b32 v20, v14
  s_nop 1
  v_max_f32_dpp v20, v20, v20 row_ror:8 row_mask:0xf bank_mask:0xf
  v_mov_b32 v21, v20
  s_nop 1
  v_permlane16_swap_b32 v20, v21
  v_max_f32 v20, v20, v21
  v_mov_b32 v21, v20
  s_nop 1
  v_permlane32_swap_b32 v20, v21
  v_max_f32 v20, v20, v21
  v_sub_f32 v21, v14, v20
  v_exp_f32 v21, v21
  s_nop 0
  v_mul_f32 v22, v21, v15
  v_mul_f32 v10, v10, v21
  v_mul_f32 v11, v11, v21
  v_add_f32_dpp v22, v22, v22 row_ror:8 row_mask:0xf bank_mask:0xf
  v_add_f32_dpp v10, v10, v10 row_ror:8 row_mask:0xf bank_mask:0xf
  v_add_f32_dpp v11, v11, v11 row_ror:8 row_mask:0xf bank_mask:0xf
  v_mov_b32 v23, v22
  s_nop 1
  v_permlane16_swap_b32 v22, v23
  v_permlane16_swap_b32 v10, v11
  v_add_f32 v22, v22, v23
  v_add_f32 v10, v10, v11
  v_mov_b32 v23, v22
  v_mov_b32 v12, v10
  s_nop 0
  v_permlane32_swap_b32 v22, v23
  v_permlane32_swap_b32 v10, v12
  v_add_f32 v22, v22, v23
  v_add_f32 v10, v10, v12
  v_rcp_f32 v22, v22
  s_nop 0
  v_mov_b32_dpp v10, v10 row_shl:4 row_mask:0xc bank_mask:0xf
  v_mul_f32 v10, v10, v22
  v_cvt_pk_bf16_f32 v18, v10, v10
  s_nop 1
  v_and_b32_dpp v10, v18, v2 row_newbcast:0 row_mask:0xf bank_mask:0xf
  v_and_b32_dpp v11, v18, v3 row_newbcast:0 row_mask:0xf bank_mask:0xf
  v_and_b32_dpp v12, v18, v4 row_newbcast:0 row_mask:0xf bank_mask:0xf
  v_and_b32_dpp v13, v18, v5 row_newbcast:0 row_mask:0xf bank_mask:0xf
  v_and_b32_dpp v14, v18, v2 row_newbcast:1 row_mask:0xf bank_mask:0xf
  v_and_b32_dpp v15, v18, v3 row_newbcast:1 row_mask:0xf bank_mask:0xf
  v_and_b32_dpp v16, v18, v4 row_newbcast:1 row_mask:0xf bank_mask:0xf
  v_and_b32_dpp v17, v18, v5 row_newbcast:1 row_mask:0xf bank_mask:0xf
  v_mfma_f32_16x16x32_bf16 v[20:23], v[24:27], v[10:13], 0
  v_and_b32_dpp v10, v18, v2 row_newbcast:2 row_mask:0xf bank_mask:0xf
  v_and_b32_dpp v11, v18, v3 row_newbcast:2 row_mask:0xf bank_mask:0xf
  v_and_b32_dpp v12, v18, v4 row_newbcast:2 row_mask:0xf bank_mask:0xf
  v_and_b32_dpp v13, v18, v5 row_newbcast:2 row_mask:0xf bank_mask:0xf
  v_mfma_f32_16x16x32_bf16 v[20:23], v[28:31], v[14:17], v[20:23]
  v_and_b32_dpp v14, v18, v2 row_newbcast:3 row_mask:0xf bank_mask:0xf
  v_and_b32_dpp v15, v18, v3 row_newbcast:3 row_mask:0xf bank_mask:0xf
  v_and_b32_dpp v16, v18, v4 row_newbcast:3 row_mask:0xf bank_mask:0xf
  v_and_b32_dpp v17, v18, v5 row_newbcast:3 row_mask:0xf bank_mask:0xf
  v_mfma_f32_16x16x32_bf16 v[20:23], v[32:35], v[10:13], v[20:23]
  v_and_b32 v9, 0xf0, v0
  v_lshlrev_b32 v9, 2, v9
  v_bfe_u32 v19, v0, 8, 2
  v_lshl_or_b32 v9, v19, 4, v9
  s_lshl_b32 s72, s65, 9
  s_add_u32 s72, s72, s70
  v_add_u32 v9, s72, v9
  v_mfma_f32_16x16x32_bf16 v[20:23], v[36:39], v[14:17], v[20:23]
  s_mov_b32 exec_lo, 0xff00ff
  s_mov_b32 exec_hi, 0xff00ff
  s_nop 5
  ds_write_b128 v9, v[20:23]
  s_mov_b64 exec, -1
  s_add_u32 s72, s95, s97
  s_waitcnt lgkmcnt(0)
  s_barrier
  s_cmp_lg_u32 s65, 1
  s_cbranch_scc1 .ya74
  s_setprio 3
  s_mov_b32 exec_lo, 0xffffffff
  s_mov_b32 exec_hi, 0xff
  ds_read_b64 v[12:13], v6 offset:0
  ds_read_b64 v[14:15], v6 offset:512
  ds_read_b64 v[16:17], v6 offset:1024
  ds_read_b64 v[18:19], v6 offset:1536
  ds_read_b64 v[20:21], v6 offset:2048
  ds_read_b64 v[22:23], v6 offset:2560
  ds_read_b64 v[24:25], v6 offset:3072
  ds_read_b64 v[26:27], v6 offset:3584
  s_mov_b32 s78, 0xffffffff
  s_mov_b32 s79, 0xff
  s_waitcnt lgkmcnt(4)
  v_add_f32 v12, v12, v14
  v_add_f32 v13, v13, v15
  v_add_f32 v16, v16, v18
  v_add_f32 v17, v17, v19
  s_waitcnt lgkmcnt(0)
  v_add_f32 v20, v20, v22
  v_add_f32 v21, v21, v23
  v_add_f32 v24, v24, v26
  v_add_f32 v25, v25, v27
  v_add_f32 v12, v12, v16
  v_add_f32 v13, v13, v17
  v_add_f32 v20, v20, v24
  v_add_f32 v21, v21, v25
  v_add_f32 v12, v12, v20
  v_add_f32 v13, v13, v21
  v_cvt_pk_bf16_f32 v16, v12, v13
  s_mov_b64 exec, -1
  v_cndmask_b32 v16, 0, v16, s[78:79]
  ds_bpermute_b32 v12, v7, v16
  ds_bpermute_b32 v13, v7, v16 offset:4
  ds_bpermute_b32 v14, v7, v16 offset:8
  v_mov_b32 v15, s62
  s_waitcnt lgkmcnt(0)
  s_mov_b32 exec_lo, 0xffff
  s_mov_b32 exec_hi, 0x0
  buffer_store_dwordx4 v[12:15], v8, s[48:51], s72 offen sc1
  s_mov_b64 exec, -1
  s_setprio 0
.ya74:
  s_xor_b32 s70, s70, 4160
  s_lshl_b32 s72, s64, 2
  s_add_u32 s72, s72, 62
  s_sub_u32 s72, s72, 64
  s_add_u32 s62, s60, s72
  s_lshr_b32 s95, s72, 1
  s_and_b32 s95, s95, 2
  s_or_b32 s95, s95, 0
  s_mul_i32 s95, s95, 589824
  s_add_u32 s62, s62, 1
  s_or_b32 s62, s62, 0x80000000
  v_bfe_u32 v22, v0, 9, 1
  v_mul_u32_u24 v22, 393216, v22
  v_bfe_u32 v23, v0, 8, 1
  v_lshl_add_u32 v22, v23, 16, v22
  v_and_b32 v23, 0xf0, v0
  v_add_u32 v22, v22, v23
  s_lshl_b32 s73, s65, 13
  s_add_u32 s74, s73, 0
  buffer_load_dwordx4 v[28:31], v22, s[44:47], s74 offen
  s_add_u32 s74, s73, 131072
  buffer_load_dwordx4 v[32:35], v22, s[44:47], s74 offen
  s_add_u32 s74, s73, 262144
  buffer_load_dwordx4 v[36:39], v22, s[44:47], s74 offen
  s_xor_b32 s72, s70, 4160
  v_bfe_u32 v8, v0, 4, 6
  v_lshlrev_b32 v6, 3, v8
  v_add_u32 v6, s72, v6
  v_and_b32 v7, 7, v8
  v_lshlrev_b32 v7, 3, v7
  v_add_u32 v7, s72, v7
  v_lshlrev_b32 v8, 4, v8
  v_bfe_u32 v9, v0, 4, 6
  v_lshlrev_b32 v23, 4, v9
  s_lshl_b32 s76, s67, 5
  s_lshl_b32 s77, s65, 2
  s_add_u32 s76, s76, s77
  s_mul_i32 s76, s76, 256
  s_add_u32 s76, s76, s95
  s_add_u32 s76, s76, 262144
  v_bfe_u32 v9, v0, 4, 4
  v_bfe_u32 v27, v0, 9, 1
  v_mul_u32_u24 v27, 3, v27
  v_add_u32 v9, v9, v27
  v_bfe_u32 v27, v0, 8, 1
  v_lshl_or_b32 v9, v9, 1, v27
  s_add_u32 s77, s64, 1
  s_and_b32 s77, s77, 1
  s_lshl_b32 s77, s77, 10
  s_add_u32 s77, s77, 158144
  s_lshl_b32 s72, s65, 6
  s_add_u32 s77, s77, s72
  v_lshlrev_b32 v27, 2, v9
  v_add_u32 v27, s77, v27
  ds_read_b32 v22, v27
  v_bfe_u32 v25, v0, 4, 4
  v_mul_u32_u24 v25, 24, v25
  v_bfe_u32 v26, v0, 8, 1
  v_lshl_add_u32 v25, v26, 2, v25
  s_mul_i32 s72, s65, 384
  s_add_u32 s72, s72, s70
  v_add_u32 v25, s72, v25
  s_mul_i32 s72, s65, 40
)ASM"
R"ASM(
  s_add_u32 s72, s72, s70
  v_lshlrev_b32 v26, 2, v9
  v_add_u32 v26, s72, v26
  s_sleep 12
  buffer_load_dwordx4 v[10:13], v23, s[48:51], s76 offen sc1
  s_waitcnt vmcnt(0)
  v_cmp_eq_u32 vcc, s62, v13
  s_nop 0
  s_mov_b64 s[78:79], vcc
  s_cmp_eq_u64 s[78:79], exec
  s_nop 1
  s_cbranch_scc1 .ok75
  s_mov_b32 s94, 0
.re76:
  s_cmp_lg_u32 s71, 0
  s_cbranch_scc1 .ok75
  s_add_u32 s94, s94, 1
  s_cmp_gt_u32 s94, 200000
  s_cselect_b32 s71, 1, 0
  buffer_load_dwordx4 v[10:13], v23, s[48:51], s76 offen sc1
  s_waitcnt vmcnt(0)
  v_cmp_eq_u32 vcc, s62, v13
  s_nop 0
  s_mov_b64 s[78:79], vcc
  s_cmp_eq_u64 s[78:79], exec
  s_nop 1
  s_cbranch_scc0 .re76
.ok75:
  v_lshlrev_b32 v14, 16, v10
  v_and_b32 v15, 0xffff0000, v10
  v_lshlrev_b32 v16, 16, v11
  v_and_b32 v17, 0xffff0000, v11
  v_lshlrev_b32 v18, 16, v12
  v_and_b32 v19, 0xffff0000, v12
  v_permlane16_swap_b32 v14, v15
  v_permlane16_swap_b32 v16, v17
  v_permlane16_swap_b32 v18, v19
  v_add_f32 v14, v14, v15
  v_add_f32 v16, v16, v17
  v_add_f32 v18, v18, v19
  v_mov_b32 v20, v14
  v_mov_b32 v21, v16
  v_mov_b32 v24, v18
  v_permlane32_swap_b32 v14, v20
  v_permlane32_swap_b32 v16, v21
  v_permlane32_swap_b32 v18, v24
  v_add_f32 v14, v14, v20
  v_add_f32 v16, v16, v21
  v_add_f32 v18, v18, v24
  s_mov_b32 exec_lo, 0x3fff3fff
  s_mov_b32 exec_hi, 0x0
  ds_write_b32 v25, v14
  ds_write_b32 v25, v16 offset:8
  ds_write_b32 v25, v18 offset:16
  s_mov_b64 exec, -1
  s_waitcnt lgkmcnt(0)
  s_barrier
  ds_read_b32 v12, v26 offset:0
  ds_read_b32 v13, v26 offset:384
  ds_read_b32 v14, v26 offset:768
  ds_read_b32 v15, v26 offset:1152
  ds_read_b32 v16, v26 offset:1536
  ds_read_b32 v17, v26 offset:1920
  ds_read_b32 v18, v26 offset:2304
  ds_read_b32 v19, v26 offset:2688
  s_waitcnt lgkmcnt(4)
  v_add_f32 v12, v12, v13
  v_add_f32 v14, v14, v15
  s_waitcnt lgkmcnt(0)
  v_add_f32 v16, v16, v17
  v_add_f32 v18, v18, v19
  v_add_f32 v12, v12, v14
  v_add_f32 v16, v16, v18
  v_add_f32 v10, v12, v16
  v_cvt_pk_bf16_f32 v10, v10, v10
  s_nop 0
  v_and_b32 v10, 0xffff0000, v10
  v_add_f32 v12, v22, v10
  v_cvt_pk_bf16_f32 v13, v12, v12
  s_nop 0
  v_and_b32 v12, 0xffff0000, v13
  s_xor_b32 s70, s70, 4160
  v_cmp_gt_u32 vcc, 10, v9
  v_mov_b32 v27, v12
  s_nop 0
  v_cndmask_b32 v24, 0, v13, vcc
  s_nop 1
  v_and_b32_dpp v10, v24, v2 row_newbcast:0 row_mask:0xf bank_mask:0xf
  v_and_b32_dpp v11, v24, v3 row_newbcast:0 row_mask:0xf bank_mask:0xf
  v_and_b32_dpp v12, v24, v4 row_newbcast:0 row_mask:0xf bank_mask:0xf
  v_and_b32_dpp v13, v24, v5 row_newbcast:0 row_mask:0xf bank_mask:0xf
  v_and_b32_dpp v14, v24, v2 row_newbcast:1 row_mask:0xf bank_mask:0xf
  v_and_b32_dpp v15, v24, v3 row_newbcast:1 row_mask:0xf bank_mask:0xf
  v_and_b32_dpp v16, v24, v4 row_newbcast:1 row_mask:0xf bank_mask:0xf
  v_and_b32_dpp v17, v24, v5 row_newbcast:1 row_mask:0xf bank_mask:0xf
  v_mfma_f32_16x16x32_bf16 v[20:23], v[28:31], v[10:13], 0
  v_and_b32_dpp v10, v24, v2 row_newbcast:2 row_mask:0xf bank_mask:0xf
  v_and_b32_dpp v11, v24, v3 row_newbcast:2 row_mask:0xf bank_mask:0xf
  v_and_b32_dpp v12, v24, v4 row_newbcast:2 row_mask:0xf bank_mask:0xf
  v_and_b32_dpp v13, v24, v5 row_newbcast:2 row_mask:0xf bank_mask:0xf
  v_mfma_f32_16x16x32_bf16 v[20:23], v[32:35], v[14:17], v[20:23]
  s_add_u32 s77, s64, 1
  s_and_b32 s77, s77, 1
  s_lshl_b32 s77, s77, 10
  s_add_u32 s77, s77, 158656
  s_lshl_b32 s72, s65, 6
  s_add_u32 s77, s77, s72
  v_lshlrev_b32 v9, 2, v9
  v_add_u32 v9, s77, v9
  v_mfma_f32_16x16x32_bf16 v[20:23], v[36:39], v[10:13], v[20:23]
  s_mov_b32 exec_lo, 0x70007
  s_mov_b32 exec_hi, 0x30003
  ds_write_b32 v9, v27
  s_mov_b64 exec, -1
  v_and_b32 v9, 0xf0, v0
  v_lshlrev_b32 v9, 2, v9
  v_bfe_u32 v25, v0, 8, 2
  v_lshl_or_b32 v9, v25, 4, v9
  s_lshl_b32 s72, s65, 9
  s_add_u32 s72, s72, s70
  v_add_u32 v9, s72, v9
  s_mov_b32 s78, 0x70007
  s_mov_b32 s79, 0x30003
  v_cndmask_b32 v16, 0, v27, s[78:79]
  v_mul_f32 v17, v16, v16
  s_nop 0
  v_add_f32_dpp v16, v16, v16 quad_perm:[1,0,3,2] row_mask:0xf bank_mask:0xf
  v_add_f32_dpp v17, v17, v17 quad_perm:[1,0,3,2] row_mask:0xf bank_mask:0xf
  s_nop 0
  v_add_f32_dpp v16, v16, v16 quad_perm:[2,3,0,1] row_mask:0xf bank_mask:0xf
  v_add_f32_dpp v17, v17, v17 quad_perm:[2,3,0,1] row_mask:0xf bank_mask:0xf
  s_nop 1
  v_permlane16_swap_b32 v16, v17
  v_add_f32 v16, v16, v17
  v_mov_b32 v18, v16
  s_nop 1
  v_permlane32_swap_b32 v16, v18
  v_add_f32 v16, v16, v18
  s_lshl_b32 s72, s65, 3
  s_add_u32 s72, s72, s70
  v_bfe_u32 v19, v0, 8, 1
  v_lshlrev_b32 v19, 2, v19
  v_add_u32 v19, s72, v19
  s_mov_b32 exec_lo, 0x10001
  s_mov_b32 exec_hi, 0x0
  ds_write_b32 v19, v16 offset:4096
  s_mov_b64 exec, -1
  s_mov_b32 exec_lo, 0xff00ff
  s_mov_b32 exec_hi, 0xff00ff
  ds_write_b128 v9, v[20:23]
  s_mov_b64 exec, -1
  s_waitcnt lgkmcnt(0)
  s_barrier
  s_cmp_lg_u32 s65, 2
  s_cbranch_scc1 .ub77
  s_setprio 3
  ds_read_b64 v[12:13], v6 offset:0
  ds_read_b64 v[14:15], v6 offset:512
  ds_read_b64 v[16:17], v6 offset:1024
  ds_read_b64 v[18:19], v6 offset:1536
  ds_read_b64 v[20:21], v6 offset:2048
  ds_read_b64 v[22:23], v6 offset:2560
  ds_read_b64 v[24:25], v6 offset:3072
  ds_read_b64 v[26:27], v6 offset:3584
  ds_read_b64 v[28:29], v7 offset:4096
  s_add_u32 s72, s95, s98
  s_waitcnt lgkmcnt(5)
  v_add_f32 v12, v12, v14
  v_add_f32 v13, v13, v15
  v_add_f32 v16, v16, v18
  v_add_f32 v17, v17, v19
  s_waitcnt lgkmcnt(1)
  v_add_f32 v20, v20, v22
  v_add_f32 v21, v21, v23
  v_add_f32 v24, v24, v26
  v_add_f32 v25, v25, v27
  v_add_f32 v12, v12, v16
  v_add_f32 v13, v13, v17
  v_add_f32 v20, v20, v24
  v_add_f32 v21, v21, v25
  v_add_f32 v12, v12, v20
  v_add_f32 v13, v13, v21
  s_waitcnt lgkmcnt(0)
  v_add_f32_dpp v28, v28, v28 quad_perm:[1,0,3,2] row_mask:0xf bank_mask:0xf
  v_add_f32_dpp v29, v29, v29 quad_perm:[1,0,3,2] row_mask:0xf bank_mask:0xf
  s_nop 0
  v_add_f32_dpp v28, v28, v28 quad_perm:[2,3,0,1] row_mask:0xf bank_mask:0xf
  v_add_f32_dpp v29, v29, v29 quad_perm:[2,3,0,1] row_mask:0xf bank_mask:0xf
  s_nop 0
  v_add_f32_dpp v28, v28, v28 row_half_mirror row_mask:0xf bank_mask:0xf
  v_add_f32_dpp v29, v29, v29 row_half_mirror row_mask:0xf bank_mask:0xf
  v_cndmask_b32 v14, v28, v29, s[80:81]
  v_mov_b32 v15, s62
  buffer_store_dwordx4 v[12:15], v8, s[48:51], s72 offen sc1
  s_setprio 0
  s_nop 0
.ub77:
  s_xor_b32 s70, s70, 4160
  s_lshl_b32 s72, s64, 2
  s_add_u32 s72, s72, 62
)ASM"
R"ASM(
  s_sub_u32 s72, s72, 64
  s_add_u32 s62, s60, s72
  s_lshr_b32 s95, s72, 1
  s_and_b32 s95, s95, 2
  s_or_b32 s95, s95, 0
  s_mul_i32 s95, s95, 589824
  s_add_u32 s62, s62, 1
  s_or_b32 s62, s62, 0x80000000
  v_bfe_u32 v9, v0, 4, 6
  v_lshrrev_b32 v20, 3, v9
  v_mul_u32_u24 v20, 1024, v20
  v_and_b32 v9, 7, v9
  v_lshl_add_u32 v20, v9, 4, v20
  s_mul_i32 s76, s66, 8192
  s_add_u32 s76, s76, s95
  s_add_u32 s76, s76, 327680
  s_lshl_b32 s73, s65, 7
  s_add_u32 s76, s76, s73
  v_bfe_u32 v18, v0, 8, 1
  v_lshl_or_b32 v19, v9, 1, v18
  v_lshlrev_b32 v19, 2, v19
  s_lshl_b32 s73, s65, 6
  s_add_u32 s73, s73, 157632
  v_add_u32 v19, s73, v19
  ds_read_b32 v22, v19
  v_lshl_or_b32 v18, v9, 1, v18
  v_lshlrev_b32 v18, 4, v18
  s_lshl_b32 s73, s65, 1
  s_add_u32 s73, s73, 155840
  v_add_u32 v18, s73, v18
  s_sleep 8
  buffer_load_dwordx4 v[10:13], v20, s[48:51], s76 offen sc1
  s_waitcnt vmcnt(0)
  v_cmp_eq_u32 vcc, s62, v13
  s_nop 0
  s_mov_b64 s[78:79], vcc
  s_cmp_eq_u64 s[78:79], exec
  s_nop 1
  s_cbranch_scc1 .ok78
  s_mov_b32 s94, 0
.re79:
  s_cmp_lg_u32 s71, 0
  s_cbranch_scc1 .ok78
  s_add_u32 s94, s94, 1
  s_cmp_gt_u32 s94, 200000
  s_cselect_b32 s71, 1, 0
  buffer_load_dwordx4 v[10:13], v20, s[48:51], s76 offen sc1
  s_waitcnt vmcnt(0)
  v_cmp_eq_u32 vcc, s62, v13
  s_nop 0
  s_mov_b64 s[78:79], vcc
  s_cmp_eq_u64 s[78:79], exec
  s_nop 1
  s_cbranch_scc0 .re79
.ok78:
  v_add_f32_dpp v12, v12, v12 row_ror:8 row_mask:0xf bank_mask:0xf
  v_add_f32_dpp v10, v10, v10 row_ror:8 row_mask:0xf bank_mask:0xf
  v_add_f32_dpp v11, v11, v11 row_ror:8 row_mask:0xf bank_mask:0xf
  v_mov_b32 v16, v12
  s_nop 1
  v_permlane16_swap_b32 v12, v16
  v_permlane16_swap_b32 v10, v11
  v_add_f32 v12, v12, v16
  v_add_f32 v10, v10, v11
  v_mov_b32 v16, v12
  v_mov_b32 v17, v10
  s_nop 0
  v_permlane32_swap_b32 v12, v16
  v_permlane32_swap_b32 v10, v17
  v_add_f32 v12, v12, v16
  v_add_f32 v10, v10, v17
  s_nop 0
  v_mov_b32_dpp v14, v12 quad_perm:[0,0,2,2] row_mask:0xf bank_mask:0xf
  v_mov_b32_dpp v15, v12 quad_perm:[1,1,3,3] row_mask:0xf bank_mask:0xf
  v_mov_b32 v16, 0x3727c5ac
  v_mul_f32 v14, 0x3acccccd, v14
  v_mul_f32 v15, 0x3acccccd, v15
  v_fma_f32 v15, -v14, v14, v15
  v_max_f32 v15, 0, v15
  v_add_f32 v15, v15, v16
  v_rsq_f32 v15, v15
  s_waitcnt lgkmcnt(0)
  v_fma_f32 v10, -v14, v22, v10
  v_mul_f32 v10, v10, v15
  v_cvt_pk_bf16_f32 v10, v10, v10
  s_mov_b32 exec_lo, 0xff00ff
  s_mov_b32 exec_hi, 0x0
  ds_write_b16 v18, v10
  s_mov_b64 exec, -1
  s_waitcnt lgkmcnt(0)
  s_barrier
  s_mov_b32 s76, 155840
  v_and_b32 v9, 0xff, v1
  v_add_u32 v9, s76, v9
  v_mov_b32 v22, 155776
  v_cndmask_b32 v23, v22, v9, s[84:85]
  ds_read_b128 v[24:27], v23
  s_nop 0
  v_cndmask_b32 v23, v22, v9, s[86:87]
  ds_read_b128 v[28:31], v23
  s_nop 0
  v_cndmask_b32 v23, v22, v9, s[88:89]
  ds_read_b128 v[32:35], v23
  s_nop 0
  v_cndmask_b32 v23, v22, v9, s[90:91]
  ds_read_b128 v[36:39], v23
  s_waitcnt lgkmcnt(0)
  v_mfma_f32_16x16x32_bf16 v[10:13], v[136:139], v[24:27], 0
  v_mfma_f32_16x16x32_bf16 v[10:13], v[140:143], v[28:31], v[10:13]
  v_mfma_f32_16x16x32_bf16 v[10:13], v[144:147], v[32:35], v[10:13]
  v_mfma_f32_16x16x32_bf16 v[10:13], v[148:151], v[36:39], v[10:13]
  v_mfma_f32_16x16x32_bf16 v[14:17], v[168:171], v[24:27], 0
  v_mfma_f32_16x16x32_bf16 v[14:17], v[172:175], v[28:31], v[14:17]
  v_mfma_f32_16x16x32_bf16 v[14:17], v[176:179], v[32:35], v[14:17]
  v_mfma_f32_16x16x32_bf16 v[14:17], v[180:183], v[36:39], v[14:17]
  s_nop 3
  v_cndmask_b32 v22, v10, v11, s[80:81]
  v_cndmask_b32 v23, v12, v13, s[80:81]
  v_mfma_f32_16x16x32_bf16 v[10:13], v[152:155], v[24:27], 0
  v_mfma_f32_16x16x32_bf16 v[10:13], v[156:159], v[28:31], v[10:13]
  v_mfma_f32_16x16x32_bf16 v[10:13], v[160:163], v[32:35], v[10:13]
  v_mfma_f32_16x16x32_bf16 v[10:13], v[164:167], v[36:39], v[10:13]
  v_cndmask_b32 v14, v14, v15, s[80:81]
  v_cndmask_b32 v16, v16, v17, s[80:81]
  v_cndmask_b32 v15, v22, v16, s[82:83]
  v_cndmask_b32 v17, v14, v23, s[82:83]
  s_nop 1
  v_add_f32_dpp v23, v17, v15 quad_perm:[2,3,0,1] row_mask:0xf bank_mask:0xf
  v_mfma_f32_16x16x32_bf16 v[14:17], v[184:187], v[24:27], 0
  v_mfma_f32_16x16x32_bf16 v[14:17], v[188:191], v[28:31], v[14:17]
  v_mfma_f32_16x16x32_bf16 v[14:17], v[192:195], v[32:35], v[14:17]
  v_mfma_f32_16x16x32_bf16 v[14:17], v[196:199], v[36:39], v[14:17]
  v_cndmask_b32 v22, v10, v11, s[80:81]
  v_cndmask_b32 v9, v12, v13, s[80:81]
  s_nop 5
  v_cndmask_b32 v14, v14, v15, s[80:81]
  v_cndmask_b32 v16, v16, v17, s[80:81]
  v_cndmask_b32 v15, v22, v16, s[82:83]
  v_cndmask_b32 v17, v14, v9, s[82:83]
  s_nop 1
  v_add_f32_dpp v22, v17, v15 quad_perm:[2,3,0,1] row_mask:0xf bank_mask:0xf
  v_cndmask_b32 v10, v23, v22, s[80:81]
  v_cndmask_b32 v11, v22, v23, s[80:81]
  s_nop 1
  v_add_f32_dpp v12, v11, v10 quad_perm:[1,0,3,2] row_mask:0xf bank_mask:0xf
  s_nop 1
  v_max_f32_dpp v13, v12, v12 row_ror:8 row_mask:0xf bank_mask:0xf
  s_nop 1
  v_max_f32_dpp v13, v13, v13 row_ror:4 row_mask:0xf bank_mask:0xf
  s_nop 1
  v_max_f32_dpp v13, v13, v13 quad_perm:[2,3,0,1] row_mask:0xf bank_mask:0xf
  s_nop 1
  v_max_f32_dpp v13, v13, v13 quad_perm:[1,0,3,2] row_mask:0xf bank_mask:0xf
  v_mov_b32 v14, v13
  s_nop 1
  v_permlane16_swap_b32 v13, v14
  v_max_f32 v13, v13, v14
  v_mov_b32 v14, v13
  s_nop 1
  v_permlane32_swap_b32 v13, v14
  v_max_f32 v13, v13, v14
  v_mul_f32 v37, s92, v13
  v_fma_f32 v14, v12, s92, -v37
  v_exp_f32 v38, v14
  s_nop 0
  v_cvt_pk_bf16_f32 v36, v38, v38
  s_nop 1
  v_and_b32_dpp v10, v36, v2 row_newbcast:0 row_mask:0xf bank_mask:0xf
  v_and_b32_dpp v11, v36, v3 row_newbcast:0 row_mask:0xf bank_mask:0xf
  v_and_b32_dpp v12, v36, v4 row_newbcast:0 row_mask:0xf bank_mask:0xf
  v_and_b32_dpp v13, v36, v5 row_newbcast:0 row_mask:0xf bank_mask:0xf
  v_and_b32_dpp v14, v36, v2 row_newbcast:4 row_mask:0xf bank_mask:0xf
  v_and_b32_dpp v15, v36, v3 row_newbcast:4 row_mask:0xf bank_mask:0xf
  v_and_b32_dpp v16, v36, v4 row_newbcast:4 row_mask:0xf bank_mask:0xf
  v_and_b32_dpp v17, v36, v5 row_newbcast:4 row_mask:0xf bank_mask:0xf
  v_mfma_f32_16x16x32_bf16 v[18:21], v[200:203], v[10:13], 0
  v_and_b32_dpp v10, v36, v2 row_newbcast:8 row_mask:0xf bank_mask:0xf
  v_and_b32_dpp v11, v36, v3 row_newbcast:8 row_mask:0xf bank_mask:0xf
  v_and_b32_dpp v12, v36, v4 row_newbcast:8 row_mask:0xf bank_mask:0xf
  v_and_b32_dpp v13, v36, v5 row_newbcast:8 row_mask:0xf bank_mask:0xf
  v_mfma_f32_16x16x32_bf16 v[18:21], v[204:207], v[14:17], v[18:21]
  v_and_b32_dpp v14, v36, v2 row_newbcast:12 row_mask:0xf bank_mask:0xf
  v_and_b32_dpp v15, v36, v3 row_newbcast:12 row_mask:0xf bank_mask:0xf
  v_and_b32_dpp v16, v36, v4 row_newbcast:12 row_mask:0xf bank_mask:0xf
  v_and_b32_dpp v17, v36, v5 row_newbcast:12 row_mask:0xf bank_mask:0xf
  v_mfma_f32_16x16x32_bf16 v[18:21], v[208:211], v[10:13], v[18:21]
  v_and_b32_dpp v10, v36, v2 row_newbcast:1 row_mask:0xf bank_mask:0xf
  v_and_b32_dpp v11, v36, v3 row_newbcast:1 row_mask:0xf bank_mask:0xf
  v_and_b32_dpp v12, v36, v4 row_newbcast:1 row_mask:0xf bank_mask:0xf
  v_and_b32_dpp v13, v36, v5 row_newbcast:1 row_mask:0xf bank_mask:0xf
  v_mfma_f32_16x16x32_bf16 v[18:21], v[212:215], v[14:17], v[18:21]
  v_and_b32_dpp v14, v36, v2 row_newbcast:5 row_mask:0xf bank_mask:0xf
  v_and_b32_dpp v15, v36, v3 row_newbcast:5 row_mask:0xf bank_mask:0xf
  v_and_b32_dpp v16, v36, v4 row_newbcast:5 row_mask:0xf bank_mask:0xf
  v_and_b32_dpp v17, v36, v5 row_newbcast:5 row_mask:0xf bank_mask:0xf
  v_mfma_f32_16x16x32_bf16 v[18:21], v[216:219], v[10:13], v[18:21]
  v_and_b32_dpp v10, v36, v2 row_newbcast:9 row_mask:0xf bank_mask:0xf
  v_and_b32_dpp v11, v36, v3 row_newbcast:9 row_mask:0xf bank_mask:0xf
  v_and_b32_dpp v12, v36, v4 row_newbcast:9 row_mask:0xf bank_mask:0xf
  v_and_b32_dpp v13, v36, v5 row_newbcast:9 row_mask:0xf bank_mask:0xf
)ASM"
R"ASM(
  v_mfma_f32_16x16x32_bf16 v[18:21], v[220:223], v[14:17], v[18:21]
  v_and_b32_dpp v14, v36, v2 row_newbcast:13 row_mask:0xf bank_mask:0xf
  v_and_b32_dpp v15, v36, v3 row_newbcast:13 row_mask:0xf bank_mask:0xf
  v_and_b32_dpp v16, v36, v4 row_newbcast:13 row_mask:0xf bank_mask:0xf
  v_and_b32_dpp v17, v36, v5 row_newbcast:13 row_mask:0xf bank_mask:0xf
  v_mfma_f32_16x16x32_bf16 v[18:21], v[224:227], v[10:13], v[18:21]
  v_and_b32_dpp v10, v36, v2 row_newbcast:2 row_mask:0xf bank_mask:0xf
  v_and_b32_dpp v11, v36, v3 row_newbcast:2 row_mask:0xf bank_mask:0xf
  v_and_b32_dpp v12, v36, v4 row_newbcast:2 row_mask:0xf bank_mask:0xf
  v_and_b32_dpp v13, v36, v5 row_newbcast:2 row_mask:0xf bank_mask:0xf
  v_mfma_f32_16x16x32_bf16 v[18:21], v[228:231], v[14:17], v[18:21]
  v_and_b32_dpp v14, v36, v2 row_newbcast:6 row_mask:0xf bank_mask:0xf
  v_and_b32_dpp v15, v36, v3 row_newbcast:6 row_mask:0xf bank_mask:0xf
  v_and_b32_dpp v16, v36, v4 row_newbcast:6 row_mask:0xf bank_mask:0xf
  v_and_b32_dpp v17, v36, v5 row_newbcast:6 row_mask:0xf bank_mask:0xf
  v_mfma_f32_16x16x32_bf16 v[18:21], v[232:235], v[10:13], v[18:21]
  v_and_b32_dpp v10, v36, v2 row_newbcast:10 row_mask:0xf bank_mask:0xf
  v_and_b32_dpp v11, v36, v3 row_newbcast:10 row_mask:0xf bank_mask:0xf
  v_and_b32_dpp v12, v36, v4 row_newbcast:10 row_mask:0xf bank_mask:0xf
  v_and_b32_dpp v13, v36, v5 row_newbcast:10 row_mask:0xf bank_mask:0xf
  v_mfma_f32_16x16x32_bf16 v[18:21], v[236:239], v[14:17], v[18:21]
  v_and_b32_dpp v14, v36, v2 row_newbcast:14 row_mask:0xf bank_mask:0xf
  v_and_b32_dpp v15, v36, v3 row_newbcast:14 row_mask:0xf bank_mask:0xf
  v_and_b32_dpp v16, v36, v4 row_newbcast:14 row_mask:0xf bank_mask:0xf
  v_and_b32_dpp v17, v36, v5 row_newbcast:14 row_mask:0xf bank_mask:0xf
  v_mfma_f32_16x16x32_bf16 v[18:21], v[240:243], v[10:13], v[18:21]
  v_and_b32_dpp v10, v36, v2 row_newbcast:3 row_mask:0xf bank_mask:0xf
  v_and_b32_dpp v11, v36, v3 row_newbcast:3 row_mask:0xf bank_mask:0xf
  v_and_b32_dpp v12, v36, v4 row_newbcast:3 row_mask:0xf bank_mask:0xf
  v_and_b32_dpp v13, v36, v5 row_newbcast:3 row_mask:0xf bank_mask:0xf
  v_mfma_f32_16x16x32_bf16 v[18:21], v[244:247], v[14:17], v[18:21]
  v_and_b32_dpp v14, v36, v2 row_newbcast:7 row_mask:0xf bank_mask:0xf
  v_and_b32_dpp v15, v36, v3 row_newbcast:7 row_mask:0xf bank_mask:0xf
  v_and_b32_dpp v16, v36, v4 row_newbcast:7 row_mask:0xf bank_mask:0xf
  v_and_b32_dpp v17, v36, v5 row_newbcast:7 row_mask:0xf bank_mask:0xf
  v_mfma_f32_16x16x32_bf16 v[18:21], v[248:251], v[10:13], v[18:21]
  v_and_b32_dpp v10, v36, v2 row_newbcast:11 row_mask:0xf bank_mask:0xf
  v_and_b32_dpp v11, v36, v3 row_newbcast:11 row_mask:0xf bank_mask:0xf
  v_and_b32_dpp v12, v36, v4 row_newbcast:11 row_mask:0xf bank_mask:0xf
  v_and_b32_dpp v13, v36, v5 row_newbcast:11 row_mask:0xf bank_mask:0xf
  v_mfma_f32_16x16x32_bf16 v[18:21], v[252:255], v[14:17], v[18:21]
  v_add_u32 v9, 131072, v0
  ds_read_b128 v[28:31], v9
  s_waitcnt lgkmcnt(0)
  v_and_b32_dpp v14, v36, v2 row_newbcast:15 row_mask:0xf bank_mask:0xf
  v_and_b32_dpp v15, v36, v3 row_newbcast:15 row_mask:0xf bank_mask:0xf
  v_and_b32_dpp v16, v36, v4 row_newbcast:15 row_mask:0xf bank_mask:0xf
  v_and_b32_dpp v17, v36, v5 row_newbcast:15 row_mask:0xf bank_mask:0xf
  v_mfma_f32_16x16x32_bf16 v[18:21], v[28:31], v[10:13], v[18:21]
  v_add_u32 v9, 139264, v0
  ds_read_b128 v[32:35], v9
  s_waitcnt lgkmcnt(0)
  v_mfma_f32_16x16x32_bf16 v[18:21], v[32:35], v[14:17], v[18:21]
  v_add_f32_dpp v22, v38, v38 row_ror:8 row_mask:0xf bank_mask:0xf
  s_nop 1
  v_add_f32_dpp v22, v22, v22 row_ror:4 row_mask:0xf bank_mask:0xf
  s_nop 1
  v_add_f32_dpp v22, v22, v22 quad_perm:[2,3,0,1] row_mask:0xf bank_mask:0xf
  s_nop 1
  v_add_f32_dpp v22, v22, v22 quad_perm:[1,0,3,2] row_mask:0xf bank_mask:0xf
  v_mov_b32 v23, v22
  s_nop 1
  v_permlane16_swap_b32 v22, v23
  v_add_f32 v22, v22, v23
  v_mov_b32 v23, v22
  s_nop 1
  v_permlane32_swap_b32 v22, v23
  v_add_f32 v22, v22, v23
  v_and_b32 v9, 0xf0, v0
  v_lshlrev_b32 v9, 2, v9
  v_bfe_u32 v10, v0, 8, 2
  v_lshl_or_b32 v9, v10, 4, v9
  s_lshl_b32 s72, s65, 9
  s_add_u32 s72, s72, s70
  v_add_u32 v9, s72, v9
  s_mov_b32 exec_lo, 0xff00ff
  s_mov_b32 exec_hi, 0xff00ff
  ds_write_b128 v9, v[18:21]
  s_mov_b32 exec_lo, 0x1
  s_mov_b32 exec_hi, 0x0
  s_lshl_b32 s72, s65, 3
  s_add_u32 s72, s72, s70
  v_mov_b32 v9, s72
  v_mov_b32 v23, v22
  v_mov_b32 v22, v37
  ds_write_b64 v9, v[22:23] offset:4096
  s_mov_b64 exec, -1
  v_bfe_u32 v9, v0, 4, 6
  v_lshlrev_b32 v28, 3, v9
  v_add_u32 v28, s70, v28
  v_and_b32 v29, 7, v9
  v_lshlrev_b32 v29, 3, v29
  v_add_u32 v29, s70, v29
  v_lshlrev_b32 v9, 4, v9
  s_lshl_b32 s72, s64, 2
  s_add_u32 s72, s72, 63
  s_sub_u32 s72, s72, 64
  s_add_u32 s62, s60, s72
  s_lshr_b32 s95, s72, 1
  s_and_b32 s95, s95, 2
  s_or_b32 s95, s95, 1
  s_mul_i32 s95, s95, 589824
  s_add_u32 s62, s62, 1
  s_or_b32 s62, s62, 0x80000000
  s_add_u32 s72, s95, s96
  s_waitcnt lgkmcnt(0)
  s_barrier
  s_cmp_lg_u32 s65, 0
  s_cbranch_scc1 .mg80
  s_setprio 3
  ds_read_b64 v[26:27], v29 offset:4096
  ds_read_b64 v[10:11], v28 offset:0
  ds_read_b64 v[12:13], v28 offset:512
  ds_read_b64 v[14:15], v28 offset:1024
  ds_read_b64 v[16:17], v28 offset:1536
  ds_read_b64 v[18:19], v28 offset:2048
  ds_read_b64 v[20:21], v28 offset:2560
  ds_read_b64 v[22:23], v28 offset:3072
  ds_read_b64 v[24:25], v28 offset:3584
  s_waitcnt lgkmcnt(8)
  v_mov_b32 v30, v26
  s_nop 1
  v_max_f32_dpp v30, v30, v30 quad_perm:[1,0,3,2] row_mask:0xf bank_mask:0xf
  s_nop 1
  v_max_f32_dpp v30, v30, v30 quad_perm:[2,3,0,1] row_mask:0xf bank_mask:0xf
  s_nop 1
  v_max_f32_dpp v30, v30, v30 row_half_mirror row_mask:0xf bank_mask:0xf
  v_sub_f32 v31, v26, v30
  v_exp_f32 v31, v31
  s_nop 0
  v_mul_f32 v32, v31, v27
  v_mov_b32 v36, 0
  v_mov_b32 v37, 0
  v_mov_b32 v39, s62
  v_add_f32_dpp v32, v32, v32 quad_perm:[1,0,3,2] row_mask:0xf bank_mask:0xf
  s_nop 1
  v_add_f32_dpp v32, v32, v32 quad_perm:[2,3,0,1] row_mask:0xf bank_mask:0xf
  s_nop 1
  v_add_f32_dpp v32, v32, v32 row_half_mirror row_mask:0xf bank_mask:0xf
  s_waitcnt lgkmcnt(0)
  v_fmac_f32_dpp v36, v31, v10 row_newbcast:0 row_mask:0xf bank_mask:0xf
  v_fmac_f32_dpp v37, v31, v11 row_newbcast:0 row_mask:0xf bank_mask:0xf
  s_nop 0
  v_fmac_f32_dpp v36, v31, v12 row_newbcast:1 row_mask:0xf bank_mask:0xf
  v_fmac_f32_dpp v37, v31, v13 row_newbcast:1 row_mask:0xf bank_mask:0xf
  s_nop 0
  v_fmac_f32_dpp v36, v31, v14 row_newbcast:2 row_mask:0xf bank_mask:0xf
  v_fmac_f32_dpp v37, v31, v15 row_newbcast:2 row_mask:0xf bank_mask:0xf
  s_nop 0
  v_fmac_f32_dpp v36, v31, v16 row_newbcast:3 row_mask:0xf bank_mask:0xf
  v_fmac_f32_dpp v37, v31, v17 row_newbcast:3 row_mask:0xf bank_mask:0xf
  s_nop 0
  v_fmac_f32_dpp v36, v31, v18 row_newbcast:4 row_mask:0xf bank_mask:0xf
  v_fmac_f32_dpp v37, v31, v19 row_newbcast:4 row_mask:0xf bank_mask:0xf
  s_nop 0
  v_fmac_f32_dpp v36, v31, v20 row_newbcast:5 row_mask:0xf bank_mask:0xf
  v_fmac_f32_dpp v37, v31, v21 row_newbcast:5 row_mask:0xf bank_mask:0xf
  s_nop 0
  v_fmac_f32_dpp v36, v31, v22 row_newbcast:6 row_mask:0xf bank_mask:0xf
  v_fmac_f32_dpp v37, v31, v23 row_newbcast:6 row_mask:0xf bank_mask:0xf
  s_nop 0
  v_fmac_f32_dpp v36, v31, v24 row_newbcast:7 row_mask:0xf bank_mask:0xf
  v_fmac_f32_dpp v37, v31, v25 row_newbcast:7 row_mask:0xf bank_mask:0xf
  v_cndmask_b32 v38, v30, v32, s[80:81]
  buffer_store_dwordx4 v[36:39], v9, s[48:51], s72 offen sc1
  s_setprio 0
  s_nop 0
.mg80:
  s_xor_b32 s70, s70, 4160
  s_lshl_b32 s72, s64, 2
  s_add_u32 s72, s72, 63
  s_sub_u32 s72, s72, 64
  s_add_u32 s62, s60, s72
  s_lshr_b32 s95, s72, 1
  s_and_b32 s95, s95, 2
  s_or_b32 s95, s95, 1
  s_mul_i32 s95, s95, 589824
  s_add_u32 s62, s62, 1
  s_or_b32 s62, s62, 0x80000000
  v_bfe_u32 v19, v0, 8, 1
  v_mul_u32_u24 v19, 10240, v19
  v_bfe_u32 v18, v0, 9, 1
  v_mul_u32_u24 v18, 81920, v18
  v_add_u32 v19, v19, v18
  v_and_b32 v18, 0xf0, v0
  v_add_u32 v19, v19, v18
  s_mul_i32 s73, s65, 1280
  s_mov_b32 exec_lo, 0x3ff03ff
  s_mov_b32 exec_hi, 0x3ff03ff
  s_add_u32 s74, s73, 0
  buffer_load_dwordx4 v[24:27], v19, s[36:39], s74 offen
  s_add_u32 s74, s73, 20480
  buffer_load_dwordx4 v[28:31], v19, s[36:39], s74 offen
  s_add_u32 s74, s73, 40960
  buffer_load_dwordx4 v[32:35], v19, s[36:39], s74 offen
  s_add_u32 s74, s73, 61440
  buffer_load_dwordx4 v[36:39], v19, s[36:39], s74 offen
  s_mov_b64 exec, -1
  v_bfe_u32 v8, v0, 4, 6
  v_mul_u32_u24 v6, 52, v8
)ASM"
R"ASM(
  v_lshrrev_b32 v6, 8, v6
  v_mul_u32_u24 v7, 5, v6
  v_sub_u32 v7, v8, v7
  v_lshlrev_b32 v6, 6, v6
  v_lshl_add_u32 v6, v7, 3, v6
  v_add_u32 v6, s70, v6
  v_mul_u32_u24 v7, 12, v8
  v_lshlrev_b32 v8, 4, v8
  v_bfe_u32 v9, v0, 4, 6
  v_lshrrev_b32 v20, 3, v9
  v_mul_u32_u24 v20, 1024, v20
  v_and_b32 v9, 7, v9
  v_lshl_add_u32 v20, v9, 4, v20
  s_mul_i32 s76, s66, 8192
  s_add_u32 s76, s76, s95
  s_lshl_b32 s73, s65, 7
  s_add_u32 s76, s76, s73
  s_sleep 8
  buffer_load_dwordx4 v[10:13], v20, s[48:51], s76 offen sc1
  s_waitcnt vmcnt(0)
  v_cmp_eq_u32 vcc, s62, v13
  s_nop 0
  s_mov_b64 s[78:79], vcc
  s_cmp_eq_u64 s[78:79], exec
  s_nop 1
  s_cbranch_scc1 .ok81
  s_mov_b32 s94, 0
.re82:
  s_cmp_lg_u32 s71, 0
  s_cbranch_scc1 .ok81
  s_add_u32 s94, s94, 1
  s_cmp_gt_u32 s94, 200000
  s_cselect_b32 s71, 1, 0
  buffer_load_dwordx4 v[10:13], v20, s[48:51], s76 offen sc1
  s_waitcnt vmcnt(0)
  v_cmp_eq_u32 vcc, s62, v13
  s_nop 0
  s_mov_b64 s[78:79], vcc
  s_cmp_eq_u64 s[78:79], exec
  s_nop 1
  s_cbranch_scc0 .re82
.ok81:
  v_mov_b32_dpp v14, v12 quad_perm:[0,0,2,2] row_mask:0xf bank_mask:0xf
  v_mov_b32_dpp v15, v12 quad_perm:[1,1,3,3] row_mask:0xf bank_mask:0xf
  v_mov_b32 v20, v14
  s_nop 1
  v_max_f32_dpp v20, v20, v20 row_ror:8 row_mask:0xf bank_mask:0xf
  v_mov_b32 v21, v20
  s_nop 1
  v_permlane16_swap_b32 v20, v21
  v_max_f32 v20, v20, v21
  v_mov_b32 v21, v20
  s_nop 1
  v_permlane32_swap_b32 v20, v21
  v_max_f32 v20, v20, v21
  v_sub_f32 v21, v14, v20
  v_exp_f32 v21, v21
  s_nop 0
  v_mul_f32 v22, v21, v15
  v_mul_f32 v10, v10, v21
  v_mul_f32 v11, v11, v21
  v_add_f32_dpp v22, v22, v22 row_ror:8 row_mask:0xf bank_mask:0xf
  v_add_f32_dpp v10, v10, v10 row_ror:8 row_mask:0xf bank_mask:0xf
  v_add_f32_dpp v11, v11, v11 row_ror:8 row_mask:0xf bank_mask:0xf
  v_mov_b32 v23, v22
  s_nop 1
  v_permlane16_swap_b32 v22, v23
  v_permlane16_swap_b32 v10, v11
  v_add_f32 v22, v22, v23
  v_add_f32 v10, v10, v11
  v_mov_b32 v23, v22
  v_mov_b32 v12, v10
  s_nop 0
  v_permlane32_swap_b32 v22, v23
  v_permlane32_swap_b32 v10, v12
  v_add_f32 v22, v22, v23
  v_add_f32 v10, v10, v12
  v_rcp_f32 v22, v22
  s_nop 0
  v_mov_b32_dpp v10, v10 row_shl:4 row_mask:0xc bank_mask:0xf
  v_mul_f32 v10, v10, v22
  v_cvt_pk_bf16_f32 v18, v10, v10
  s_nop 1
  v_and_b32_dpp v10, v18, v2 row_newbcast:0 row_mask:0xf bank_mask:0xf
  v_and_b32_dpp v11, v18, v3 row_newbcast:0 row_mask:0xf bank_mask:0xf
  v_and_b32_dpp v12, v18, v4 row_newbcast:0 row_mask:0xf bank_mask:0xf
  v_and_b32_dpp v13, v18, v5 row_newbcast:0 row_mask:0xf bank_mask:0xf
  v_and_b32_dpp v14, v18, v2 row_newbcast:1 row_mask:0xf bank_mask:0xf
  v_and_b32_dpp v15, v18, v3 row_newbcast:1 row_mask:0xf bank_mask:0xf
  v_and_b32_dpp v16, v18, v4 row_newbcast:1 row_mask:0xf bank_mask:0xf
  v_and_b32_dpp v17, v18, v5 row_newbcast:1 row_mask:0xf bank_mask:0xf
  v_mfma_f32_16x16x32_bf16 v[20:23], v[24:27], v[10:13], 0
  v_and_b32_dpp v10, v18, v2 row_newbcast:2 row_mask:0xf bank_mask:0xf
  v_and_b32_dpp v11, v18, v3 row_newbcast:2 row_mask:0xf bank_mask:0xf
  v_and_b32_dpp v12, v18, v4 row_newbcast:2 row_mask:0xf bank_mask:0xf
  v_and_b32_dpp v13, v18, v5 row_newbcast:2 row_mask:0xf bank_mask:0xf
  v_mfma_f32_16x16x32_bf16 v[20:23], v[28:31], v[14:17], v[20:23]
  v_and_b32_dpp v14, v18, v2 row_newbcast:3 row_mask:0xf bank_mask:0xf
  v_and_b32_dpp v15, v18, v3 row_newbcast:3 row_mask:0xf bank_mask:0xf
  v_and_b32_dpp v16, v18, v4 row_newbcast:3 row_mask:0xf bank_mask:0xf
  v_and_b32_dpp v17, v18, v5 row_newbcast:3 row_mask:0xf bank_mask:0xf
  v_mfma_f32_16x16x32_bf16 v[20:23], v[32:35], v[10:13], v[20:23]
  v_and_b32 v9, 0xf0, v0
  v_lshlrev_b32 v9, 2, v9
  v_bfe_u32 v19, v0, 8, 2
  v_lshl_or_b32 v9, v19, 4, v9
  s_lshl_b32 s72, s65, 9
  s_add_u32 s72, s72, s70
  v_add_u32 v9, s72, v9
  v_mfma_f32_16x16x32_bf16 v[20:23], v[36:39], v[14:17], v[20:23]
  s_mov_b32 exec_lo, 0xff00ff
  s_mov_b32 exec_hi, 0xff00ff
  s_nop 5
  ds_write_b128 v9, v[20:23]
  s_mov_b64 exec, -1
  s_add_u32 s72, s95, s97
  s_waitcnt lgkmcnt(0)
  s_barrier
  s_cmp_lg_u32 s65, 1
  s_cbranch_scc1 .ya83
  s_setprio 3
  s_mov_b32 exec_lo, 0xffffffff
  s_mov_b32 exec_hi, 0xff
  ds_read_b64 v[12:13], v6 offset:0
  ds_read_b64 v[14:15], v6 offset:512
  ds_read_b64 v[16:17], v6 offset:1024
  ds_read_b64 v[18:19], v6 offset:1536
  ds_read_b64 v[20:21], v6 offset:2048
  ds_read_b64 v[22:23], v6 offset:2560
  ds_read_b64 v[24:25], v6 offset:3072
  ds_read_b64 v[26:27], v6 offset:3584
  s_mov_b32 s78, 0xffffffff
  s_mov_b32 s79, 0xff
  s_waitcnt lgkmcnt(4)
  v_add_f32 v12, v12, v14
  v_add_f32 v13, v13, v15
  v_add_f32 v16, v16, v18
  v_add_f32 v17, v17, v19
  s_waitcnt lgkmcnt(0)
  v_add_f32 v20, v20, v22
  v_add_f32 v21, v21, v23
  v_add_f32 v24, v24, v26
  v_add_f32 v25, v25, v27
  v_add_f32 v12, v12, v16
  v_add_f32 v13, v13, v17
  v_add_f32 v20, v20, v24
  v_add_f32 v21, v21, v25
  v_add_f32 v12, v12, v20
  v_add_f32 v13, v13, v21
  v_cvt_pk_bf16_f32 v16, v12, v13
  s_mov_b64 exec, -1
  v_cndmask_b32 v16, 0, v16, s[78:79]
  ds_bpermute_b32 v12, v7, v16
  ds_bpermute_b32 v13, v7, v16 offset:4
  ds_bpermute_b32 v14, v7, v16 offset:8
  v_mov_b32 v15, s62
  s_waitcnt lgkmcnt(0)
  s_mov_b32 exec_lo, 0xffff
  s_mov_b32 exec_hi, 0x0
  buffer_store_dwordx4 v[12:15], v8, s[48:51], s72 offen sc1
  s_mov_b64 exec, -1
  s_setprio 0
.ya83:
  s_xor_b32 s70, s70, 4160
  s_cmp_lg_u32 s66, 0
  s_cbranch_scc1 .bf84
  s_lshl_b32 s72, s64, 2
  s_add_u32 s72, s72, 63
  s_sub_u32 s72, s72, 64
  s_add_u32 s62, s60, s72
  s_lshr_b32 s95, s72, 1
  s_and_b32 s95, s95, 2
  s_or_b32 s95, s95, 1
  s_mul_i32 s95, s95, 589824
  s_add_u32 s62, s62, 1
  s_or_b32 s62, s62, 0x80000000
  v_bfe_u32 v9, v0, 4, 6
  v_lshlrev_b32 v23, 4, v9
  s_lshl_b32 s76, s67, 5
  s_lshl_b32 s77, s65, 2
  s_add_u32 s76, s76, s77
  s_mul_i32 s76, s76, 256
  s_add_u32 s76, s76, s95
  s_add_u32 s76, s76, 262144
  v_bfe_u32 v9, v0, 4, 4
  v_bfe_u32 v27, v0, 9, 1
  v_mul_u32_u24 v27, 3, v27
  v_add_u32 v9, v9, v27
  v_bfe_u32 v27, v0, 8, 1
  v_lshl_or_b32 v9, v9, 1, v27
  s_add_u32 s77, s64, 1
  s_and_b32 s77, s77, 1
  s_lshl_b32 s77, s77, 10
  s_add_u32 s77, s77, 158656
  s_lshl_b32 s72, s65, 6
  s_add_u32 s77, s77, s72
  v_lshlrev_b32 v27, 2, v9
  v_add_u32 v27, s77, v27
  ds_read_b32 v22, v27
  v_bfe_u32 v25, v0, 4, 4
)ASM"
R"ASM(
  v_mul_u32_u24 v25, 24, v25
  v_bfe_u32 v26, v0, 8, 1
  v_lshl_add_u32 v25, v26, 2, v25
  s_mul_i32 s72, s65, 384
  s_add_u32 s72, s72, s70
  v_add_u32 v25, s72, v25
  s_mul_i32 s72, s65, 40
  s_add_u32 s72, s72, s70
  v_lshlrev_b32 v26, 2, v9
  v_add_u32 v26, s72, v26
  s_sleep 12
  buffer_load_dwordx4 v[10:13], v23, s[48:51], s76 offen sc1
  s_waitcnt vmcnt(0)
  v_cmp_eq_u32 vcc, s62, v13
  s_nop 0
  s_mov_b64 s[78:79], vcc
  s_cmp_eq_u64 s[78:79], exec
  s_nop 1
  s_cbranch_scc1 .ok86
  s_mov_b32 s94, 0
.re87:
  s_cmp_lg_u32 s71, 0
  s_cbranch_scc1 .ok86
  s_add_u32 s94, s94, 1
  s_cmp_gt_u32 s94, 200000
  s_cselect_b32 s71, 1, 0
  buffer_load_dwordx4 v[10:13], v23, s[48:51], s76 offen sc1
  s_waitcnt vmcnt(0)
  v_cmp_eq_u32 vcc, s62, v13
  s_nop 0
  s_mov_b64 s[78:79], vcc
  s_cmp_eq_u64 s[78:79], exec
  s_nop 1
  s_cbranch_scc0 .re87
.ok86:
  v_lshlrev_b32 v14, 16, v10
  v_and_b32 v15, 0xffff0000, v10
  v_lshlrev_b32 v16, 16, v11
  v_and_b32 v17, 0xffff0000, v11
  v_lshlrev_b32 v18, 16, v12
  v_and_b32 v19, 0xffff0000, v12
  v_permlane16_swap_b32 v14, v15
  v_permlane16_swap_b32 v16, v17
  v_permlane16_swap_b32 v18, v19
  v_add_f32 v14, v14, v15
  v_add_f32 v16, v16, v17
  v_add_f32 v18, v18, v19
  v_mov_b32 v20, v14
  v_mov_b32 v21, v16
  v_mov_b32 v24, v18
  v_permlane32_swap_b32 v14, v20
  v_permlane32_swap_b32 v16, v21
  v_permlane32_swap_b32 v18, v24
  v_add_f32 v14, v14, v20
  v_add_f32 v16, v16, v21
  v_add_f32 v18, v18, v24
  s_mov_b32 exec_lo, 0x3fff3fff
  s_mov_b32 exec_hi, 0x0
  ds_write_b32 v25, v14
  ds_write_b32 v25, v16 offset:8
  ds_write_b32 v25, v18 offset:16
  s_mov_b64 exec, -1
  s_waitcnt lgkmcnt(0)
  s_barrier
  ds_read_b32 v12, v26 offset:0
  ds_read_b32 v13, v26 offset:384
  ds_read_b32 v14, v26 offset:768
  ds_read_b32 v15, v26 offset:1152
  ds_read_b32 v16, v26 offset:1536
  ds_read_b32 v17, v26 offset:1920
  ds_read_b32 v18, v26 offset:2304
  ds_read_b32 v19, v26 offset:2688
  s_waitcnt lgkmcnt(4)
  v_add_f32 v12, v12, v13
  v_add_f32 v14, v14, v15
  s_waitcnt lgkmcnt(0)
  v_add_f32 v16, v16, v17
  v_add_f32 v18, v18, v19
  v_add_f32 v12, v12, v14
  v_add_f32 v16, v16, v18
  v_add_f32 v10, v12, v16
  v_cvt_pk_bf16_f32 v10, v10, v10
  s_nop 0
  v_and_b32 v10, 0xffff0000, v10
  v_add_f32 v12, v22, v10
  v_cvt_pk_bf16_f32 v13, v12, v12
  s_nop 0
  v_and_b32 v12, 0xffff0000, v13
  v_lshlrev_b32 v9, 4, v9
  s_add_u32 s72, s64, 3
  s_sub_u32 s72, s72, 4
  s_mul_i32 s72, s72, 1280
  s_mul_i32 s73, s67, 160
  s_add_u32 s72, s72, s73
  s_lshl_b32 s73, s65, 1
  s_add_u32 s72, s72, s73
  s_mov_b32 exec_lo, 0x70007
  s_mov_b32 exec_hi, 0x30003
  buffer_store_short v13, v9, s[52:55], s72 offen
  s_mov_b64 exec, -1
  s_xor_b32 s70, s70, 4160
.bf84:
  s_cmp_lg_u32 s99, 0
  s_cbranch_scc1 .ep88
  s_cmp_lg_u32 s65, 0
  s_cbranch_scc1 .ep88
  s_add_u32 s72, s60, 64
  v_mov_b32 v10, s72
  v_mov_b32 v11, s61
  v_mov_b32 v12, 0
  s_mov_b32 s72, 5898240
  s_mov_b32 exec_lo, 0x1
  s_mov_b32 exec_hi, 0x0
  buffer_store_dwordx2 v[10:11], v12, s[48:51], s72 offen sc1
.ep88:
  s_waitcnt vmcnt(0)
  s_endpgm
.Lkend:

)ASM"
  :: "s"(ka), "s"(blockIdx.x), "s"(wid) : "v0", "v1", "v2", "v3", "v4", "v5", "v6", "v7", "v8", "v9", "v10", "v11", "v12", "v13", "v14", "v15", "v16", "v17", "v18", "v19", "v20", "v21", "v22", "v23", "v24", "v25", "v26", "v27", "v28", "v29", "v30", "v31", "v32", "v33", "v34", "v35", "v36", "v37", "v38", "v39", "v40", "v41", "v42", "v43", "v44", "v45", "v46", "v47", "v48", "v49", "v50", "v51", "v52", "v53", "v54", "v55", "v56", "v57", "v58", "v59", "v60", "v61", "v62", "v63", "v64", "v65", "v66", "v67", "v68", "v69", "v70", "v71", "v72", "v73", "v74", "v75", "v76", "v77", "v78", "v79", "v80", "v81", "v82", "v83", "v84", "v85", "v86", "v87", "v88", "v89", "v90", "v91", "v92", "v93", "v94", "v95", "v96", "v97", "v98", "v99", "v100", "v101", "v102", "v103", "v104", "v105", "v106", "v107", "v108", "v109", "v110", "v111", "v112", "v113", "v114", "v115", "v116", "v117", "v118", "v119", "v120", "v121", "v122", "v123", "v124", "v125", "v126", "v127", "v128", "v129", "v130", "v131", "v132", "v133", "v134", "v135", "v136", "v137", "v138", "v139", "v140", "v141", "v142", "v143", "v144", "v145", "v146", "v147", "v148", "v149", "v150", "v151", "v152", "v153", "v154", "v155", "v156", "v157", "v158", "v159", "v160", "v161", "v162", "v163", "v164", "v165", "v166", "v167", "v168", "v169", "v170", "v171", "v172", "v173", "v174", "v175", "v176", "v177", "v178", "v179", "v180", "v181", "v182", "v183", "v184", "v185", "v186", "v187", "v188", "v189", "v190", "v191", "v192", "v193", "v194", "v195", "v196", "v197", "v198", "v199", "v200", "v201", "v202", "v203", "v204", "v205", "v206", "v207", "v208", "v209", "v210", "v211", "v212", "v213", "v214", "v215", "v216", "v217", "v218", "v219", "v220", "v221", "v222", "v223", "v224", "v225", "v226", "v227", "v228", "v229", "v230", "v231", "v232", "v233", "v234", "v235", "v236", "v237", "v238", "v239", "v240", "v241", "v242", "v243", "v244", "v245", "v246", "v247", "v248", "v249", "v250", "v251", "v252", "v253", "v254", "v255", "s4", "s5", "s6", "s7", "s8", "s9", "s10", "s11", "s12", "s13", "s14", "s15", "s16", "s17", "s18", "s19", "s20", "s21", "s22", "s23", "s24", "s25", "s26", "s27", "s28", "s29", "s30", "s31", "s34", "s35", "s36", "s37", "s38", "s39", "s40", "s41", "s42", "s43", "s44", "s45", "s46", "s47", "s48", "s49", "s50", "s51", "s52", "s53", "s54", "s55", "s56", "s57", "s58", "s59", "s60", "s61", "s62", "s63", "s64", "s65", "s66", "s67", "s68", "s69", "s70", "s71", "s72", "s73", "s74", "s75", "s76", "s77", "s78", "s79", "s80", "s81", "s82", "s83", "s84", "s85", "s86", "s87", "s88", "s89", "s90", "s91", "s92", "s93", "s94", "s95", "s96", "s97", "s98", "s99", "vcc", "memory");
}

extern "C" void kernel_launch(void* const* d_in, const int* in_sizes, int n_in, void* d_out, int out_size, void* d_ws, size_t ws_size, hipStream_t stream) {
  KArgs a;
  a.p[0] = d_in[0]; a.p[1] = d_in[1]; a.p[2] = d_in[2]; a.p[3] = d_in[3]; a.p[4] = d_in[4];
  a.p[5] = d_in[6]; a.p[6] = d_in[7]; a.p[7] = d_in[8]; a.p[8] = d_in[9]; a.p[9] = d_out; a.p[10] = d_ws;
  decode4_kernel<<<256, 512, 0, stream>>>(a);
}
```
